```python
import torch

def _softmax(x, axis=-1):
    return torch.softmax(x, dim=axis)

def _gelu(x):
    return torch.nn.functional.gelu(x, approximate='tanh')

def _silu(x):
    return torch.nn.functional.silu(x)

def _sigmoid(x):
    return torch.sigmoid(x)

def _layernorm(x, axis=-1, eps=1e-05):
    m = x.mean(dim=axis, keepdim=True)
    v = x.var(dim=axis, keepdim=True, unbiased=False)
    return (x - m) / torch.sqrt(v + eps)

def _scatter_add(tgt, idx, src, axis=0):
    return tgt.clone().index_add_(axis, idx, src)

def _segment_sum(src, seg, n):
    out = torch.zeros((n,) + src.shape[1:], dtype=src.dtype)
    return out.index_add_(0, seg, src)

def _pad(x, pw):
    flat = tuple((v for pair in reversed(pw) for v in pair))
    return torch.nn.functional.pad(x, flat)
kernel_name = 'concat_heads__memory_aug_recurrent_mem_tr__linear_kernel_hedgehog__prompt'

def setup_inputs():

    _w = lambda a: (lambda t: t.to(torch.bfloat16) if t.is_floating_point() else t)(torch.as_tensor(a))

    def _A_setup():
        B, S, Dm = (1, 2048, 448)
        H, Dh = (16, 256)
        M, seg = (8, 32)
        seg = min(seg, S)
        S = S // seg * seg
        return {'x': _w((lambda *s: torch.randn(*s, dtype=torch.bfloat16) * 0.05)(B, S, Dm)), 'mem0': _w((lambda *s: torch.randn(*s, dtype=torch.bfloat16) * 0.8)(B, M, Dm)), 'Wq': _w((lambda *s: torch.randn(*s, dtype=torch.bfloat16) * 1.6)(Dm, H, Dh) * Dm ** (-0.5)), 'Wk': _w((lambda *s: torch.randn(*s, dtype=torch.bfloat16) * 1.6)(Dm, H, Dh) * Dm ** (-0.5)), 'Wv': _w((lambda *s: torch.randn(*s, dtype=torch.bfloat16) * 0.8)(Dm, H, Dh) * Dm ** (-0.5)), 'Wo': _w((lambda *s: torch.randn(*s, dtype=torch.bfloat16) * 0.8)(H, Dh, Dm) * (H * Dh) ** (-0.5)), 'seg_len': seg}
    _A_in = _A_setup()

    def _B_setup():
        B, S, D, H = (1, 2048, 448, 16)
        Dh = D // H
        return {'x': _w((lambda *s: torch.randn(*s, dtype=torch.bfloat16) * 0.4)(B, S, D)), 'wqkv': _w((lambda *s: torch.randn(*s, dtype=torch.bfloat16) * 0.4)(D, 3 * D)), 'w_phi': _w((lambda *s: torch.randn(*s, dtype=torch.bfloat16) * 0.4)(H, Dh, Dh) * Dh ** (-0.5)), 'wo': _w((lambda *s: torch.randn(*s, dtype=torch.bfloat16) * 0.05)(D, D))}
    _B_in = _B_setup()
    return {'A_x': _A_in['x'], 'A_mem0': _A_in['mem0'], 'A_Wq': _A_in['Wq'], 'A_Wk': _A_in['Wk'], 'A_Wv': _A_in['Wv'], 'A_Wo': _A_in['Wo'], 'A_seg_len': _A_in['seg_len'], 'B_x': _B_in['x'], 'B_wqkv': _B_in['wqkv'], 'B_w_phi': _B_in['w_phi'], 'B_wo': _B_in['wo']}

def reference(A_x, A_mem0, A_Wq, A_Wk, A_Wv, A_Wo, A_seg_len, B_x, B_wqkv, B_w_phi, B_wo):

    def _A_ref(x, mem0, Wq, Wk, Wv, Wo, seg_len):
        B, S, Dm = x.shape
        H, Dh = (Wq.shape[1], Wq.shape[2])
        M = mem0.shape[1]
        scale = Dh ** (-0.5)
        n_seg = S // seg_len
        L = M + seg_len
        idx = torch.arange(L)
        mask = (idx[:, None] < M) | (idx[:, None] >= idx[None, :])
        mem = mem0
        outs = []
        for s in range(n_seg):
            xs = x[:, s * seg_len:(s + 1) * seg_len, :].clone()
            h = torch.concatenate([mem, xs], axis=1)
            q = torch.einsum('btd,dhe->bhte', h, Wq)
            k = torch.einsum('btd,dhe->bhte', h, Wk)
            v = torch.einsum('btd,dhe->bhte', h, Wv)
            scores = torch.einsum('bhqe,bhke->bhqk', q, k) * scale
            scores = torch.where(mask[None, None], scores, -1000000000.0)
            p = _softmax(scores, axis=-1)
            o = torch.einsum('bhqk,bhke->bhqe', p, v)
            o = torch.einsum('bhte,hed->btd', o, Wo)
            mem = o[:, :M, :].clone()
            outs.append(o[:, M:, :])
        return torch.concatenate(outs, axis=1)

    def _B_ref(x, wqkv, w_phi, wo):
        B, S, D = x.shape
        H = 16
        Dh = D // H
        qkv = (lambda x, *d: x.permute(*d))((x @ wqkv).reshape(B, S, 3, H, Dh), 2, 0, 3, 1, 4)
        q, k, v = (qkv[0], qkv[1], qkv[2])
        pq = torch.einsum('bhsd,hdf->bhsf', q, w_phi)
        pk = torch.einsum('bhsd,hdf->bhsf', k, w_phi)
        phi_q = torch.concatenate([_softmax(pq, axis=-1), _softmax(-pq, axis=-1)], axis=-1)
        phi_k = torch.concatenate([_softmax(pk, axis=-1), _softmax(-pk, axis=-1)], axis=-1)
        kv = torch.einsum('bhsf,bhsd->bhfd', phi_k, v)
        z = torch.einsum('bhsf->bhf', phi_k)
        num = torch.einsum('bhqf,bhfd->bhqd', phi_q, kv)
        den = torch.einsum('bhqf,bhf->bhq', phi_q, z)[..., None] + 1e-06
        o = num / den
        return (lambda x, *d: x.permute(*d))(o, 0, 2, 1, 3).reshape(B, S, D) @ wo
    a = _A_ref(A_x, A_mem0, A_Wq, A_Wk, A_Wv, A_Wo, A_seg_len)
    b = _B_ref(B_x, B_wqkv, B_w_phi, B_wo)
    return torch.concatenate([a, b], axis=-1)
```

```cpp
#include <hip/hip_runtime.h>
#include <hip/hip_bf16.h>

typedef __bf16 bf16x2 __attribute__((ext_vector_type(2)));
typedef __bf16 bf16x4 __attribute__((ext_vector_type(4)));
typedef __bf16 bf16x8 __attribute__((ext_vector_type(8)));
typedef __bf16 bf16x16 __attribute__((ext_vector_type(16)));
typedef short s16x4 __attribute__((ext_vector_type(4)));
typedef float f32x4 __attribute__((ext_vector_type(4)));
typedef float f32x2 __attribute__((ext_vector_type(2)));
typedef unsigned u32x4 __attribute__((ext_vector_type(4)));
typedef unsigned u32x2 __attribute__((ext_vector_type(2)));
#define LAS __attribute__((address_space(3)))
#define GAS __attribute__((address_space(1)))
#define DI __device__ __forceinline__
#define RLX_AGENT __ATOMIC_RELAXED, __HIP_MEMORY_SCOPE_AGENT
typedef GAS unsigned gu32;

constexpr int DM = 448, NH = 16, DH = 256, SEG = 32, NSEG = 64, SEQ = 2048;
constexpr int KS = 14;
constexpr int NT = 28;
constexpr int OUTW = 896;
constexpr float LOG2E = 1.4426950408889634f;

constexpr size_t TILE_B = 14336;
constexpr size_t OFF_CTL = 0;
constexpr size_t CTL_BYTES = 65536;
constexpr size_t OFF_ATC = OFF_CTL + CTL_BYTES;
constexpr size_t OFF_WB = OFF_ATC + 448 * TILE_B;
constexpr size_t OFF_XF = OFF_WB + 1344 * TILE_B;
constexpr size_t OFF_KQ = OFF_XF + 128 * TILE_B;
constexpr size_t OFF_TX = OFF_KQ + 2048 * TILE_B;
constexpr size_t OFF_VO = OFF_TX + 2048 * TILE_B;
constexpr size_t OFF_U = OFF_VO + 2048 * TILE_B;
constexpr size_t OFF_BXF = OFF_U + (size_t)64 * 4 * 28 * 1024;
constexpr size_t OFF_BW = OFF_BXF + 128 * TILE_B;
constexpr size_t OFF_BWO = OFF_BW + 96 * TILE_B;
constexpr size_t OFF_BKVP = OFF_BWO + (size_t)28 * 16 * 1024;
constexpr size_t OFF_BKV = OFF_BKVP + (size_t)16 * 16 * 8 * 1024;
constexpr size_t OFF_END = OFF_BKV + (size_t)16 * 4 * 1024;
constexpr int NUNIT = 320;
constexpr size_t COMM_BYTES = (size_t)NUNIT * 16384;
constexpr int NBLK = (NUNIT + 15) / 16;

constexpr int CTL_ABORT = 0;
constexpr int CTL_PAR = 64;

DI unsigned pk2(float a, float b) { bf16x2 p = {(__bf16)a, (__bf16)b}; return __builtin_bit_cast(unsigned, p); }
DI f32x4 mfma32(bf16x8 a, bf16x8 b, f32x4 c) { return __builtin_amdgcn_mfma_f32_16x16x32_bf16(a, b, c, 0, 0, 0); }
DI f32x4 mfma16(u32x2 a, u32x2 b, f32x4 c) { return __builtin_amdgcn_mfma_f32_16x16x16bf16_1k(__builtin_bit_cast(s16x4, a), __builtin_bit_cast(s16x4, b), c, 0, 0, 0); }
DI f32x4 smf(bf16x8 a, bf16x16 b, f32x4 c, int idx) { return __builtin_amdgcn_smfmac_f32_16x16x64_bf16(a, b, c, idx, 0, 0); }
DI bf16x8 as8(u32x4 v) { return __builtin_bit_cast(bf16x8, v); }
DI bf16x16 cat16(bf16x8 a, bf16x8 b) { return __builtin_shufflevector(a, b, 0, 1, 2, 3, 4, 5, 6, 7, 8, 9, 10, 11, 12, 13, 14, 15); }
DI float bf2f(unsigned short v) { return __uint_as_float((unsigned)v << 16); }
DI void lds_barrier() { asm volatile("s_waitcnt lgkmcnt(0)\n\ts_barrier" ::: "memory"); }
DI unsigned long long memtime() { unsigned long long t; asm volatile("s_memtime %0\n\ts_waitcnt lgkmcnt(0)" : "=s"(t)::"memory"); return t; }
template <int CTRL> DI float dppf(float x) { return __builtin_amdgcn_update_dpp(0.f, x, CTRL, 0xf, 0xf, true); }
template <int CTRL> DI unsigned dppu(unsigned x) { return (unsigned)__builtin_amdgcn_update_dpp(0, (int)x, CTRL, 0xf, 0xf, true); }
DI float xg_max(float x) {
    auto a = __builtin_amdgcn_permlane16_swap(__float_as_uint(x), __float_as_uint(x), false, false);
    x = fmaxf(__uint_as_float(a[0]), __uint_as_float(a[1]));
    auto b = __builtin_amdgcn_permlane32_swap(__float_as_uint(x), __float_as_uint(x), false, false);
    return fmaxf(__uint_as_float(b[0]), __uint_as_float(b[1]));
}
DI float xg_sum(float x) {
    auto a = __builtin_amdgcn_permlane16_swap(__float_as_uint(x), __float_as_uint(x), false, false);
    x = __uint_as_float(a[0]) + __uint_as_float(a[1]);
    auto b = __builtin_amdgcn_permlane32_swap(__float_as_uint(x), __float_as_uint(x), false, false);
    return __uint_as_float(b[0]) + __uint_as_float(b[1]);
}
DI float swap_add(float x, float y) {
    auto b = __builtin_amdgcn_permlane32_swap(__float_as_uint(x), __float_as_uint(y), false, false);
    return __uint_as_float(b[0]) + __uint_as_float(b[1]);
}
DI float row_max(float x) {
    x = fmaxf(x, dppf<0xB1>(x)); x = fmaxf(x, dppf<0x4E>(x)); x = fmaxf(x, dppf<0x124>(x)); x = fmaxf(x, dppf<0x128>(x)); return x;
}
DI float row_sum(float x) {
    x += dppf<0xB1>(x); x += dppf<0x4E>(x); x += dppf<0x124>(x); x += dppf<0x128>(x); return x;
}

struct Params {
    const unsigned short* x; const unsigned short* mem0; const unsigned short* Wq; const unsigned short* Wk;
    const unsigned short* Wv; const unsigned short* Wo; const unsigned short* bx; const unsigned short* bwqkv;
    const unsigned short* bwphi; const unsigned short* bwo; unsigned short* out; char* ws; char* comm;
};

constexpr int K0_FOLD_WGS = 192;
constexpr int K0_MISC_WGS = 64;
__global__ void __launch_bounds__(448) k0_prep(Params p) {
    extern __shared__ __attribute__((aligned(16))) char smem[];
    const int tid = threadIdx.x, lane = tid & 63, wave = __builtin_amdgcn_readfirstlane(tid >> 6);
    const int b = blockIdx.x;
    if (b < K0_FOLD_WGS) {
        const int type = b / 64, h = (b % 64) >> 2, nq = b & 3;
        const unsigned short* Q = type == 0 ? p.Wk : (type == 1 ? p.Wq : p.Wv);
        constexpr int WO_STRIDE = 240;
        if (type < 2) {
            const unsigned short* P = type == 0 ? p.Wq : p.Wk;
#pragma unroll
            for (int i = 0; i < 8; ++i) {
                const int c = tid + 448 * i;
                const int row = c >> 5, ch = c & 31;
                const u32x4 v = *(const u32x4*)(P + ((size_t)(112 * nq + row) * NH + h) * DH + ch * 8);
                const int tl = row >> 4, n = row & 15, es = ch >> 2, g = ch & 3;
                *(u32x4*)(smem + ((tl * 8 + es) * 64 + g * 16 + n) * 16) = v;
            }
        } else {
#pragma unroll
            for (int i = 0; i < 8; ++i) {
                const int c = tid + 448 * i;
                const int row = c / 14, ch = c % 14;
                const u32x4 v = *(const u32x4*)(p.Wo + ((size_t)h * DH + row) * DM + 112 * nq + ch * 8);
                *(u32x4*)(smem + row * WO_STRIDE + ch * 16) = v;
            }
        }
        __syncthreads();
        const int r16 = lane & 15, g = lane >> 4;
        f32x4 acc[4][7];
#pragma unroll
        for (int a = 0; a < 4; ++a)
#pragma unroll
            for (int t = 0; t < 7; ++t) acc[a][t] = (f32x4){0.f, 0.f, 0.f, 0.f};
        const unsigned short* qp[4];
#pragma unroll
        for (int a = 0; a < 4; ++a) {
            const int k = 64 * wave + 32 * (a >> 1) + 8 * (r16 >> 2) + 4 * (a & 1) + (r16 & 3);
            qp[a] = Q + ((size_t)k * NH + h) * DH + 8 * g;
        }
#pragma unroll 2
        for (int es = 0; es < 8; ++es) {
            bf16x8 qa[4];
#pragma unroll
            for (int a = 0; a < 4; ++a) qa[a] = as8(*(const u32x4*)(qp[a] + 32 * es));
#pragma unroll
            for (int t = 0; t < 7; ++t) {
                bf16x8 pb;
                if (type < 2) {
                    pb = as8(*(const u32x4*)(smem + ((t * 8 + es) * 64 + lane) * 16));
                } else {
                    const int q = r16 >> 2, pp = r16 & 3;
                    const char* base = smem + (32 * es + 8 * g + q) * WO_STRIDE + 32 * t + 8 * pp;
                    bf16x4 lo = __builtin_amdgcn_ds_read_tr16_b64_v4bf16((LAS bf16x4*)(base));
                    bf16x4 hi = __builtin_amdgcn_ds_read_tr16_b64_v4bf16((LAS bf16x4*)(base + 4 * WO_STRIDE));
                    pb = __builtin_shufflevector(lo, hi, 0, 1, 2, 3, 4, 5, 6, 7);
                }
#pragma unroll
                for (int a = 0; a < 4; ++a) acc[a][t] = mfma32(qa[a], pb, acc[a][t]);
            }
        }
        const float sc = type < 2 ? (0.0625f * LOG2E) : 1.0f;
#pragma unroll
        for (int t = 0; t < 7; ++t) {
            const int n = 112 * nq + 16 * t + r16;
            const int tile_nat = n >> 4;
            const int tile_p = 2 * (n >> 5) + ((n >> 2) & 1), row_p = 4 * ((n >> 3) & 3) + (n & 3);
#pragma unroll
            for (int pr = 0; pr < 2; ++pr) {
                const f32x4 lo = acc[2 * pr][t], hi = acc[2 * pr + 1][t];
                const u32x4 v = {pk2(lo[0] * sc, lo[1] * sc), pk2(lo[2] * sc, lo[3] * sc), pk2(hi[0] * sc, hi[1] * sc), pk2(hi[2] * sc, hi[3] * sc)};
                const int ks = 2 * wave + pr;
                if (type == 1) *(u32x4*)(p.ws + OFF_ATC + (size_t)(h * NT + tile_nat) * TILE_B + ks * 1024 + lane * 16) = v;
                if (type < 2) *(u32x4*)(p.ws + OFF_WB + (size_t)((type * NH + h) * NT + tile_p) * TILE_B + ks * 1024 + (g * 16 + row_p) * 16) = v;
                else *(u32x4*)(p.ws + OFF_WB + (size_t)((2 * NH + h) * NT + tile_nat) * TILE_B + ks * 1024 + lane * 16) = v;
            }
        }
        return;
    }
    const int mb = b - K0_FOLD_WGS;
    const int gt = mb * 448 + tid, gn = K0_MISC_WGS * 448;
    for (int c = gt; c < 2 * 128 * KS * 64; c += gn) {
        const int which = c / (128 * KS * 64), cc = c % (128 * KS * 64);
        const int l = cc & 63, ks = (cc >> 6) % KS, tile = (cc >> 6) / KS;
        const unsigned short* src = which ? p.bx : p.x;
        const u32x4 v = *(const u32x4*)(src + (size_t)(16 * tile + (l & 15)) * DM + 32 * ks + 8 * (l >> 4));
        *(u32x4*)(p.ws + (which ? OFF_BXF : OFF_XF) + (size_t)cc * 16) = v;
    }
    for (size_t c = gt; c < COMM_BYTES / 16; c += gn) {
        u32x4 v = {0xffffffffu, 0xffffffffu, 0xffffffffu, 0xffffffffu};
        if (c < 1024) {
            const int o = (int)(c & 511);
            if (o < 448) {
                const int cs = o >> 4, ig = (o >> 3) & 1, hf = (o >> 2) & 1, i4 = o & 3;
                const unsigned* src = (const unsigned*)(p.mem0 + (size_t)(4 * ig + i4) * DM + 16 * cs + 2 * hf);
                v = (u32x4){src[0], src[2], src[4], src[6]};
#pragma unroll
                for (int j = 0; j < 4; ++j) if (v[j] == 0xffffffffu) v[j] = 0x7fc07fc0u;
            }
        }
        *(u32x4*)(p.comm + c * 16) = v;
    }
    for (int c = gt; c < 96 * KS * 64; c += gn) {
        const int l = c & 63, ks = (c >> 6) % KS, tile = (c >> 6) / KS;
        const int type = tile >> 5, h = (tile >> 1) & 15, f = 16 * (tile & 1) + (l & 15), d0 = 32 * ks + 8 * (l >> 4);
        float o[8];
#pragma unroll
        for (int j = 0; j < 8; ++j) o[j] = 0.f;
        if (f < 28) {
            if (type == 2) {
#pragma unroll
                for (int j = 0; j < 8; ++j) o[j] = bf2f(p.bwqkv[(size_t)(d0 + j) * 1344 + 896 + 28 * h + f]);
            } else {
                for (int e = 0; e < 28; ++e) {
                    const float w = bf2f(p.bwphi[(h * 28 + e) * 28 + f]);
#pragma unroll
                    for (int j = 0; j < 8; ++j) o[j] += bf2f(p.bwqkv[(size_t)(d0 + j) * 1344 + 448 * type + 28 * h + e]) * w;
                }
#pragma unroll
                for (int j = 0; j < 8; ++j) o[j] *= LOG2E;
            }
        }
        *(u32x4*)(p.ws + OFF_BW + (size_t)c * 16) = (u32x4){pk2(o[0], o[1]), pk2(o[2], o[3]), pk2(o[4], o[5]), pk2(o[6], o[7])};
    }
    for (int c = gt; c < 28 * 16 * 64; c += gn) {
        const int l = c & 63, h = (c >> 6) & 15, ct = c >> 10;
        unsigned short o[8];
#pragma unroll
        for (int j = 0; j < 8; ++j) {
            const int e = 16 * (j >> 2) + 4 * (l >> 4) + (j & 3);
            o[j] = e < 28 ? p.bwo[(size_t)(28 * h + e) * DM + 16 * ct + (l & 15)] : (unsigned short)0;
        }
        *(u32x4*)(p.ws + OFF_BWO + (size_t)c * 16) = (u32x4){o[0] | ((unsigned)o[1] << 16), o[2] | ((unsigned)o[3] << 16), o[4] | ((unsigned)o[5] << 16), o[6] | ((unsigned)o[7] << 16)};
    }
}

__global__ void __launch_bounds__(256) kg_gemm(Params p) {
    extern __shared__ __attribute__((aligned(16))) char smem[];
    const int tid = threadIdx.x, lane = tid & 63, wave = __builtin_amdgcn_readfirstlane(tid >> 6);
    const int tile0 = 8 * blockIdx.x + 2 * wave;
    const int type = tile0 / 448, h = (tile0 % 448) / NT, t = tile0 % NT;
    bf16x8 W[2][KS];
#pragma unroll
    for (int a = 0; a < 2; ++a)
#pragma unroll
        for (int ks = 0; ks < KS; ++ks) W[a][ks] = as8(*(const u32x4*)(p.ws + OFF_WB + (size_t)(tile0 + a) * TILE_B + ks * 1024 + lane * 16));
    u32x4 st[7];
#pragma unroll
    for (int i = 0; i < 7; ++i) st[i] = *(const u32x4*)(p.ws + OFF_XF + (size_t)(tid + 256 * i) * 16);
#pragma unroll
    for (int i = 0; i < 7; ++i) *(u32x4*)(smem + (tid + 256 * i) * 16) = st[i];
    __syncthreads();
    for (int seg = 0; seg < NSEG; ++seg) {
        const char* xb = smem + (seg & 1) * 28672;
        if (seg + 1 < NSEG) {
#pragma unroll
            for (int i = 0; i < 7; ++i) st[i] = *(const u32x4*)(p.ws + OFF_XF + (size_t)(seg + 1) * 28672 + (size_t)(tid + 256 * i) * 16);
        }
        f32x4 acc[2][2];
#pragma unroll
        for (int a = 0; a < 2; ++a)
#pragma unroll
            for (int m = 0; m < 2; ++m) acc[a][m] = (f32x4){0.f, 0.f, 0.f, 0.f};
        if (type < 2) {
#pragma unroll
            for (int ks = 0; ks < KS; ++ks)
#pragma unroll
                for (int m = 0; m < 2; ++m) {
                    const bf16x8 xf = as8(*(const u32x4*)(xb + (m * KS + ks) * 1024 + lane * 16));
#pragma unroll
                    for (int a = 0; a < 2; ++a) acc[a][m] = mfma32(W[a][ks], xf, acc[a][m]);
                }
#pragma unroll
            for (int m = 0; m < 2; ++m) {
                const f32x4 lo = acc[0][m], hi = acc[1][m];
                const u32x4 v = {pk2(lo[0], lo[1]), pk2(lo[2], lo[3]), pk2(hi[0], hi[1]), pk2(hi[2], hi[3])};
                *(u32x4*)(p.ws + (type == 0 ? OFF_KQ : OFF_TX) + (size_t)((seg * NH + h) * 2 + m) * TILE_B + (t >> 1) * 1024 + lane * 16) = v;
            }
        } else {
#pragma unroll
            for (int ks = 0; ks < KS; ++ks)
#pragma unroll
                for (int m = 0; m < 2; ++m) {
                    const bf16x8 xf = as8(*(const u32x4*)(xb + (m * KS + ks) * 1024 + lane * 16));
#pragma unroll
                    for (int a = 0; a < 2; ++a) acc[a][m] = mfma32(xf, W[a][ks], acc[a][m]);
                }
#pragma unroll
            for (int a = 0; a < 2; ++a) {
                const f32x4 lo = acc[a][0], hi = acc[a][1];
                const u32x4 v = {pk2(lo[0], lo[1]), pk2(lo[2], lo[3]), pk2(hi[0], hi[1]), pk2(hi[2], hi[3])};
                *(u32x4*)(p.ws + OFF_VO + (size_t)((seg * NH + h) * NT + t + a) * 1024 + lane * 16) = v;
            }
        }
        if (seg + 1 < NSEG) {
#pragma unroll
            for (int i = 0; i < 7; ++i) *(u32x4*)(smem + ((seg + 1) & 1) * 28672 + (tid + 256 * i) * 16) = st[i];
        }
        __syncthreads();
    }
}

#define DLY_SCORE 1100
#define DLY_OUTMEM 150
#define DLY_SMM 0
constexpr unsigned SPIN_CAP = 1u << 20;
constexpr int NSTEP = 63;

DI bool has_sent4(u32x4 v) { return max(max(v[0], v[1]), max(v[2], v[3])) == 0xffffffffu; }
DI bool has_sent2(u32x2 v) { return max(v[0], v[1]) == 0xffffffffu; }
DI unsigned nosent(unsigned v) { return v == 0xffffffffu ? 0x7fc07fc0u : v; }
DI unsigned unit_off(unsigned u, unsigned parmask) { return u * 16384u + ((parmask >> (u >> 4)) & 1u) * 8192u; }
DI bool spin_fail(unsigned& spins, gu32* ctl) {
    ++spins;
    if (spins > SPIN_CAP) { __hip_atomic_store(ctl + CTL_ABORT, 1u, RLX_AGENT); return true; }
    if ((spins & 63u) == 0u) return __builtin_amdgcn_readfirstlane(__hip_atomic_load(ctl + CTL_ABORT, RLX_AGENT)) != 0;
    return false;
}
DI void wait_until(unsigned long long t) { while ((long long)(memtime() - t) < 0) {} }

__global__ void __launch_bounds__(256, 1) k_chain(Params p) {
    extern __shared__ __attribute__((aligned(16))) char smem[];
    const int tid = threadIdx.x, lane = tid & 63, wave = __builtin_amdgcn_readfirstlane(tid >> 6);
    const int xcd = blockIdx.x & 7, slot = blockIdx.x >> 3;
    if (xcd >= 4) return;
    gu32* ctl = (gu32*)(p.ws + OFF_CTL);
    const __amdgpu_buffer_rsrc_t comm = __builtin_amdgcn_make_buffer_rsrc(p.comm, 0, (int)COMM_BYTES, 0x00020000);
    const int r16 = lane & 15, g = lane >> 4;
    bool dead = false;

    if (slot >= 30) {
        const int blk = ((slot - 30) * 4 + xcd) * 4 + wave;
        if (blk < NBLK) {
            const unsigned u = ((16 * blk + 4) / 5) * 5;
            unsigned best[2] = {0xffffffffu, 0xffffffffu};
#pragma unroll
            for (int rep = 0; rep < 3; ++rep)
#pragma unroll
                for (int par = 0; par < 2; ++par) {
                    const char* q = p.comm + (size_t)u * 16384 + par * 8192 + 7168 + rep * 256;
                    unsigned v;
                    const unsigned long long t0 = memtime();
                    asm volatile("global_load_dword %0, %1, off sc1\n\ts_waitcnt vmcnt(0)" : "=v"(v) : "v"(q) : "memory");
                    const unsigned long long t1 = memtime();
                    best[par] = min(best[par], (unsigned)(t1 - t0));
                }
            if (lane == 0) __hip_atomic_store(ctl + CTL_PAR + blk, best[1] < best[0] ? 2u : 1u, RLX_AGENT);
        }
        return;
    }
    const int sidx = (r16 & 8) ? 0xEEEE : 0x4444;
    const unsigned aoff = g * 256 + ((r16 >> 2) & 1) * 128 + (r16 >> 3) * 64 + (r16 & 3) * 16;
    const int k0 = (2 * wave) % 7, k1 = (2 * wave + 1) % 7;

    if (slot < 16) {
        const int h = 4 * xcd + (slot >> 2), q = slot & 3;
        char* MEML = smem;
        char* LT = smem + 8192;
        const bool kqw = (wave == 3) && (q < 2);
        bf16x16 W[2][7];
        {
            const int tA = 7 * q + (wave < 3 ? 2 * wave : 6), tB = 7 * q + (wave < 3 ? 2 * wave + 1 : 6);
#pragma unroll
            for (int sl = 0; sl < 7; ++sl) {
                const int kap = (2 * wave + sl) % 7;
                const char* pa = p.ws + OFF_ATC + (size_t)(h * NT + tA) * TILE_B + kap * 2048 + lane * 16;
                const char* pb = kqw ? p.ws + OFF_KQ + (size_t)((0 * NH + h) * 2 + q) * TILE_B + kap * 2048 + lane * 16
                                     : p.ws + OFF_ATC + (size_t)(h * NT + tB) * TILE_B + kap * 2048 + lane * 16;
                W[0][sl] = cat16(as8(*(const u32x4*)pa), as8(*(const u32x4*)(pa + 1024)));
                W[1][sl] = cat16(as8(*(const u32x4*)pb), as8(*(const u32x4*)(pb + 1024)));
            }
        }
        for (int i = tid; i < 1024; i += 256) ((unsigned*)LT)[i] = 0u;
        unsigned parmask;
        {
            unsigned spins = 0;
            for (;;) {
                const unsigned v = __hip_atomic_load(ctl + CTL_PAR + min(lane, NBLK - 1), RLX_AGENT);
                parmask = (unsigned)__ballot(v == 2u);
                if (__all(v != 0u) || dead) break;
                dead = spin_fail(spins, ctl);
            }
        }
        __syncthreads();
        const int tlA = wave < 3 ? 2 * wave : 6, tlB = 2 * wave + 1;
        for (int s = 0; s < NSTEP; ++s) {
            const unsigned mem_o = unit_off(5 * s, parmask);
            f32x4 acc0, acc1;
            u32x4 a0, a1;
            unsigned spins = 0;
            for (;;) {
                a0 = __builtin_amdgcn_raw_buffer_load_b128(comm, mem_o + k0 * 1024 + aoff, 0, 16);
                a1 = __builtin_amdgcn_raw_buffer_load_b128(comm, mem_o + k1 * 1024 + aoff, 0, 16);
                *(u32x4*)(MEML + k0 * 1024 + lane * 16) = a0;
                *(u32x4*)(MEML + k1 * 1024 + lane * 16) = a1;
                acc1 = smf(as8(a0), W[1][0], (f32x4){0.f, 0.f, 0.f, 0.f}, sidx);
                acc0 = smf(as8(a0), W[0][0], (f32x4){0.f, 0.f, 0.f, 0.f}, sidx);
                acc1 = smf(as8(a1), W[1][1], acc1, sidx);
                acc0 = smf(as8(a1), W[0][1], acc0, sidx);
                const bool bad = has_sent4(a0) | has_sent4(a1);
                if (__builtin_expect(!__any(bad), 1) || dead) break;
                dead = spin_fail(spins, ctl);
            }
            lds_barrier();
            bf16x8 af[5];
#pragma unroll
            for (int sl = 2; sl < 7; ++sl) af[sl - 2] = as8(*(const u32x4*)(MEML + ((2 * wave + sl) % 7) * 1024 + lane * 16));
            bf16x8 kf[2];
            if (wave == 2) {
#pragma unroll
                for (int sig = 0; sig < 2; ++sig) {
                    const int tau = min(7 * q + 4 * sig + g, NT - 1);
                    kf[sig] = as8(*(const u32x4*)(MEML + (tau >> 2) * 1024 + ((tau & 3) * 16 + r16) * 16));
                }
            }
#pragma unroll
            for (int sl = 2; sl < 7; ++sl) acc1 = smf(af[sl - 2], W[1][sl], acc1, sidx);
            if (wave == 3) {
                if (kqw) {
                    const float v0 = swap_add(acc1[0], acc1[1]);
                    const float v1 = swap_add(acc1[2], acc1[3]);
                    const unsigned o = unit_off(5 * s + 3 + (g & 1), parmask) + (h * 2 + q) * 256 + (g >> 1) * 64 + r16 * 4;
                    __builtin_amdgcn_raw_buffer_store_b32(nosent(__float_as_uint(v0)), comm, o, 0, 16);
                    __builtin_amdgcn_raw_buffer_store_b32(nosent(__float_as_uint(v1)), comm, o + 128, 0, 16);
                }
            } else {
                *(u32x2*)(LT + (16 * tlB + r16) * 32 + 8 * g) = (u32x2){pk2(acc1[0], acc1[1]), pk2(acc1[2], acc1[3])};
            }
#pragma unroll
            for (int sl = 2; sl < 7; ++sl) acc0 = smf(af[sl - 2], W[0][sl], acc0, sidx);
            *(u32x2*)(LT + (16 * tlA + r16) * 32 + 8 * g) = (u32x2){pk2(acc0[0], acc0[1]), pk2(acc0[2], acc0[3])};
            lds_barrier();
            const unsigned long long tref = memtime();
            if (wave == 2) {
                f32x4 sacc = {0.f, 0.f, 0.f, 0.f};
                const char* tb = LT + (8 * g + (r16 >> 2)) * 32 + 8 * (r16 & 3);
#pragma unroll
                for (int sig = 0; sig < 2; ++sig) {
                    const bf16x4 b0 = __builtin_amdgcn_ds_read_tr16_b64_v4bf16((LAS bf16x4*)(tb + sig * 2048));
                    const bf16x4 b1 = __builtin_amdgcn_ds_read_tr16_b64_v4bf16((LAS bf16x4*)(tb + sig * 2048 + 128));
                    const bf16x4 b2 = __builtin_amdgcn_ds_read_tr16_b64_v4bf16((LAS bf16x4*)(tb + sig * 2048 + 1024));
                    const bf16x4 b3 = __builtin_amdgcn_ds_read_tr16_b64_v4bf16((LAS bf16x4*)(tb + sig * 2048 + 1024 + 128));
                    const bf16x16 bb = cat16(__builtin_shufflevector(b0, b1, 0, 1, 2, 3, 4, 5, 6, 7), __builtin_shufflevector(b2, b3, 0, 1, 2, 3, 4, 5, 6, 7));
                    sacc = smf(kf[sig], bb, sacc, sidx);
                }
                float v0 = swap_add(sacc[0], sacc[2]);
                float v1 = swap_add(sacc[1], sacc[3]);
                v0 += dppf<0x128>(v0);
                v1 += dppf<0x128>(v1);
                if (r16 < 8) {
                    const unsigned a = 2 * (g & 1) + (g >> 1);
                    const unsigned o = unit_off(5 * s + 1 + (r16 >> 2), parmask) + (h * 4 + q) * 128 + (r16 & 3) * 32 + a * 8;
                    __builtin_amdgcn_raw_buffer_store_b64((u32x2){nosent(__float_as_uint(v0)), nosent(__float_as_uint(v1))}, comm, o, 0, 16);
                }
            }
            if (kqw && s + 1 < NSTEP) {
#pragma unroll
                for (int sl = 0; sl < 7; ++sl) {
                    const int kap = (6 + sl) % 7;
                    const char* pb = p.ws + OFF_KQ + (size_t)(((s + 1) * NH + h) * 2 + q) * TILE_B + kap * 2048 + lane * 16;
                    W[1][sl] = cat16(as8(*(const u32x4*)pb), as8(*(const u32x4*)(pb + 1024)));
                }
            }
            wait_until(tref + DLY_SCORE);
        }
        return;
    }
    {
        const int os = slot - 16, ig = os & 1, cs = 7 * xcd + (os >> 1);
        char* MEML = smem;
        char* UL = smem + 8192 + wave * 1024;
        char* ML = smem + 12288 + wave * 128;
        float* RED = (float*)(smem + 13312);
        bf16x16 W[4][7];
#pragma unroll
        for (int hl = 0; hl < 4; ++hl)
#pragma unroll
            for (int sl = 0; sl < 7; ++sl) {
                const int kap = (2 * wave + sl) % 7;
                const char* pa = p.ws + OFF_WB + (size_t)((2 * NH + 4 * wave + hl) * NT + cs) * TILE_B + kap * 2048 + lane * 16;
                W[hl][sl] = cat16(as8(*(const u32x4*)pa), as8(*(const u32x4*)(pa + 1024)));
            }
        bf16x8 vo[4];
#pragma unroll
        for (int hl = 0; hl < 4; ++hl) vo[hl] = as8(*(const u32x4*)(p.ws + OFF_VO + (size_t)((0 * NH + 4 * wave + hl) * NT + cs) * 1024 + lane * 16));
        unsigned parmask;
        {
            unsigned spins = 0;
            for (;;) {
                const unsigned v = __hip_atomic_load(ctl + CTL_PAR + min(lane, NBLK - 1), RLX_AGENT);
                parmask = (unsigned)__ballot(v == 2u);
                if (__all(v != 0u) || dead) break;
                dead = spin_fail(spins, ctl);
            }
        }
        for (int s = 0; s <= NSTEP; ++s) {
            const unsigned mem_o = unit_off(5 * s, parmask);
            f32x4 acc[4];
            u32x4 a0, a1;
            unsigned spins = 0;
            for (;;) {
                a0 = __builtin_amdgcn_raw_buffer_load_b128(comm, mem_o + k0 * 1024 + aoff, 0, 16);
                a1 = __builtin_amdgcn_raw_buffer_load_b128(comm, mem_o + k1 * 1024 + aoff, 0, 16);
                *(u32x4*)(MEML + k0 * 1024 + lane * 16) = a0;
                *(u32x4*)(MEML + k1 * 1024 + lane * 16) = a1;
                const bool bad = has_sent4(a0) | has_sent4(a1);
                if (__builtin_expect(!__any(bad), 1) || dead) break;
                dead = spin_fail(spins, ctl);
            }
            const unsigned long long tmem = memtime();
            lds_barrier();
            bf16x8 af[5];
#pragma unroll
            for (int sl = 2; sl < 7; ++sl) af[sl - 2] = as8(*(const u32x4*)(MEML + ((2 * wave + sl) % 7) * 1024 + lane * 16));
#pragma unroll
            for (int hl = 0; hl < 4; ++hl) {
                acc[hl] = smf(as8(a0), W[hl][0], (f32x4){0.f, 0.f, 0.f, 0.f}, sidx);
                acc[hl] = smf(as8(a1), W[hl][1], acc[hl], sidx);
            }
#pragma unroll
            for (int hl = 0; hl < 4; ++hl)
#pragma unroll
                for (int sl = 2; sl < 7; ++sl) acc[hl] = smf(af[sl - 2], W[hl][sl], acc[hl], sidx);
            u32x4 uu;
#pragma unroll
            for (int pr = 0; pr < 2; ++pr) {
                float t[4];
#pragma unroll
                for (int r = 0; r < 4; ++r) t[r] = swap_add(acc[2 * pr][r], acc[2 * pr + 1][r]);
                uu[2 * pr] = pk2(t[0], t[1]);
                uu[2 * pr + 1] = pk2(t[2], t[3]);
                *(u32x2*)(UL + ((2 * pr + (g >> 1)) * 16 + r16) * 16 + (g & 1) * 8) = (u32x2){uu[2 * pr], uu[2 * pr + 1]};
            }
            if (ig == 0) *(u32x4*)(p.ws + OFF_U + (size_t)((s * 4 + wave) * NT + cs) * 1024 + lane * 16) = uu;
            if (s == NSTEP) break;
            asm volatile("s_waitcnt lgkmcnt(0)" ::: "memory");
            const bf16x8 ub = as8(*(const u32x4*)(UL + lane * 16));
            u32x4 x0, x1;
            {
                const unsigned o = unit_off(5 * s + 3 + ig, parmask) + ((4 * wave + (r16 >> 2)) * 2) * 256 + (((r16 & 3) >> 1) * 128 + (r16 & 1) * 64) + g * 16;
                spins = 0;
                for (;;) {
                    x0 = __builtin_amdgcn_raw_buffer_load_b128(comm, o, 0, 16);
                    x1 = __builtin_amdgcn_raw_buffer_load_b128(comm, o + 256, 0, 16);
                    const bool bad = has_sent4(x0) | has_sent4(x1);
                    if (__builtin_expect(!__any(bad), 1) || dead) break;
                    dead = spin_fail(spins, ctl);
                }
            }
            float sx[8];
#pragma unroll
            for (int j = 0; j < 4; ++j) { sx[j] = __uint_as_float(x0[j]); sx[4 + j] = __uint_as_float(x1[j]); }
            float mx = fmaxf(fmaxf(fmaxf(sx[0], sx[1]), fmaxf(sx[2], sx[3])), fmaxf(fmaxf(sx[4], sx[5]), fmaxf(sx[6], sx[7])));
            mx = xg_max(mx);
            float lx = 0.f;
#pragma unroll
            for (int j = 0; j < 8; ++j) { sx[j] = __builtin_amdgcn_exp2f(sx[j] - mx); }
            const u32x4 ex = {pk2(sx[0], sx[1]), pk2(sx[2], sx[3]), pk2(sx[4], sx[5]), pk2(sx[6], sx[7])};
#pragma unroll
            for (int j = 0; j < 8; ++j) lx += sx[j];
            lx = xg_sum(lx);
            if (g == 0) *(f32x2*)(ML + r16 * 8) = (f32x2){mx, lx};
            f32x4 ox = {0.f, 0.f, 0.f, 0.f};
#pragma unroll
            for (int hl = 0; hl < 4; ++hl) {
                const bool mine = (r16 >> 2) == hl;
                const u32x4 am = {mine ? ex[0] : 0u, mine ? ex[1] : 0u, mine ? ex[2] : 0u, mine ? ex[3] : 0u};
                ox = mfma32(as8(am), vo[hl], ox);
            }
            const u32x2 oxb = {pk2(ox[0], ox[1]), pk2(ox[2], ox[3])};
            asm volatile("s_waitcnt lgkmcnt(0)" ::: "memory");
            const f32x2 ml = *(const f32x2*)(ML + (4 * g + (r16 >> 2)) * 8);
            wait_until(tmem + DLY_SMM);
            u32x2 sp[4];
            {
                const unsigned o = unit_off(5 * s + 1 + ig, parmask) + (4 * wave + g) * 512 + r16 * 8;
                spins = 0;
                for (;;) {
#pragma unroll
                    for (int qq = 0; qq < 4; ++qq) sp[qq] = __builtin_amdgcn_raw_buffer_load_b64(comm, o + qq * 128, 0, 16);
                    const bool bad = has_sent2(sp[0]) | has_sent2(sp[1]) | has_sent2(sp[2]) | has_sent2(sp[3]);
                    if (__builtin_expect(!__any(bad), 1) || dead) break;
                    dead = spin_fail(spins, ctl);
                }
            }
            const float s0 = (__uint_as_float(sp[0][0]) + __uint_as_float(sp[1][0])) + (__uint_as_float(sp[2][0]) + __uint_as_float(sp[3][0]));
            const float s1 = (__uint_as_float(sp[0][1]) + __uint_as_float(sp[1][1])) + (__uint_as_float(sp[2][1]) + __uint_as_float(sp[3][1]));
            float m = fmaxf(fmaxf(s0, s1), ml[0]);
            m = fmaxf(m, dppf<0xB1>(m));
            m = fmaxf(m, dppf<0x4E>(m));
            const float e0 = __builtin_amdgcn_exp2f(s0 - m), e1 = __builtin_amdgcn_exp2f(s1 - m), al = __builtin_amdgcn_exp2f(ml[0] - m);
            float z = e0 + e1;
            z += dppf<0xB1>(z);
            z += dppf<0x4E>(z);
            z += al * ml[1];
            const float rz = __builtin_amdgcn_rcpf(z);
            const unsigned pp = pk2(e0 * rz, e1 * rz);
            const u32x4 a1f = {dppu<0x00>(pp), dppu<0x55>(pp), dppu<0xAA>(pp), dppu<0xFF>(pp)};
            const unsigned bb = pk2(al * rz, al * rz);
            const int i4 = r16 >> 2;
            const u32x2 a2f = {i4 == 0 ? (bb & 0xffffu) : (i4 == 1 ? (bb & 0xffff0000u) : 0u), i4 == 2 ? (bb & 0xffffu) : (i4 == 3 ? (bb & 0xffff0000u) : 0u)};
            f32x4 o = mfma32(as8(a1f), ub, (f32x4){0.f, 0.f, 0.f, 0.f});
            o = mfma32(as8((u32x4){a2f[0], a2f[1], 0u, 0u}), as8((u32x4){oxb[0], oxb[1], 0u, 0u}), o);
            RED[wave * 64 + lane] = o[0];
            lds_barrier();
            if (wave == 0 && lane < 32) {
                const int i4o = lane >> 3, cp = lane & 7;
                f32x2 t = *(const f32x2*)(RED + i4o * 16 + 2 * cp);
#pragma unroll
                for (int w = 1; w < 4; ++w) t += *(const f32x2*)(RED + w * 64 + i4o * 16 + 2 * cp);
                const unsigned oo = unit_off(5 * (s + 1), parmask) + cs * 256 + ig * 128 + (cp & 1) * 64 + i4o * 16 + (cp >> 1) * 4;
                __builtin_amdgcn_raw_buffer_store_b32(nosent(pk2(t[0], t[1])), comm, oo, 0, 16);
            }
            const unsigned long long tpub = memtime();
            if (s + 1 < NSTEP) {
#pragma unroll
                for (int hl = 0; hl < 4; ++hl) vo[hl] = as8(*(const u32x4*)(p.ws + OFF_VO + (size_t)(((s + 1) * NH + 4 * wave + hl) * NT + cs) * 1024 + lane * 16));
            }
            wait_until(tpub + DLY_OUTMEM);
        }
    }
}

__global__ void __launch_bounds__(256, 1) k_final(Params p) {
    extern __shared__ __attribute__((aligned(16))) char smem[];
    const int tid = threadIdx.x, lane = tid & 63, wave = __builtin_amdgcn_readfirstlane(tid >> 6);
    const int seg = blockIdx.x >> 1, mt = blockIdx.x & 1;
    const int r16 = lane & 15, g = lane >> 4;
    gu32* ctl = (gu32*)(p.ws + OFF_CTL);
    const unsigned parmask = (unsigned)__ballot(ctl[CTL_PAR + min(lane, NBLK - 1)] == 2u);
    char* KL = smem;
    for (int i = tid; i < 2 * KS * 64; i += 256) *(u32x4*)(KL + TILE_B + i * 16) = *(const u32x4*)(p.ws + OFF_XF + (size_t)seg * 28672 + (size_t)i * 16);
    {
        const char* mp = p.comm + unit_off(5 * seg, parmask);
        for (int i = tid; i < KS * 64 * 4; i += 256) {
            const int dw = i & 3, l = (i >> 2) & 63, ks = i >> 8;
            const int j = l & 15, d = 32 * ks + 8 * (l >> 4) + 2 * dw;
            unsigned v = 0;
            if (j < 8) v = *(const unsigned*)(mp + (d >> 4) * 256 + (j >> 2) * 128 + ((d >> 1) & 1) * 64 + (j & 3) * 16 + ((d >> 2) & 3) * 4);
            *(unsigned*)(KL + i * 4) = v;
        }
    }
    __syncthreads();
    f32x4 oacc[NT];
#pragma unroll
    for (int c = 0; c < NT; ++c) oacc[c] = (f32x4){0.f, 0.f, 0.f, 0.f};
    u32x2 pm[4];
    const int qrow = 16 * mt + r16;
#pragma unroll
    for (int hl = 0; hl < 4; ++hl) {
        const int h = 4 * wave + hl;
        f32x4 sa[3];
#pragma unroll
        for (int kt = 0; kt < 3; ++kt) sa[kt] = (f32x4){0.f, 0.f, 0.f, 0.f};
        const char* tp = p.ws + OFF_TX + (size_t)((seg * NH + h) * 2 + mt) * TILE_B + lane * 16;
#pragma unroll
        for (int ks = 0; ks < KS; ++ks) {
            const bf16x8 tf = as8(*(const u32x4*)(tp + ks * 1024));
#pragma unroll
            for (int kt = 0; kt < 3; ++kt) sa[kt] = mfma32(as8(*(const u32x4*)(KL + kt * TILE_B + ks * 1024 + lane * 16)), tf, sa[kt]);
        }
        float mx = -3.0e38f;
#pragma unroll
        for (int r = 0; r < 4; ++r) {
            if (g >= 2) sa[0][r] = -3.0e38f;
            if (4 * g + r > qrow) sa[1][r] = -3.0e38f;
            if (16 + 4 * g + r > qrow) sa[2][r] = -3.0e38f;
            mx = fmaxf(mx, fmaxf(sa[0][r], fmaxf(sa[1][r], sa[2][r])));
        }
        mx = xg_max(mx);
        float sum = 0.f;
#pragma unroll
        for (int kt = 0; kt < 3; ++kt)
#pragma unroll
            for (int r = 0; r < 4; ++r) { sa[kt][r] = __builtin_amdgcn_exp2f(sa[kt][r] - mx); sum += sa[kt][r]; }
        sum = xg_sum(sum);
        const float rs = 1.0f / sum;
        pm[hl] = (u32x2){pk2(sa[0][0] * rs, sa[0][1] * rs), pk2(sa[0][2] * rs, sa[0][3] * rs)};
        const u32x4 px = {pk2(sa[1][0] * rs, sa[1][1] * rs), pk2(sa[1][2] * rs, sa[1][3] * rs), pk2(sa[2][0] * rs, sa[2][1] * rs), pk2(sa[2][2] * rs, sa[2][3] * rs)};
        const char* vp = p.ws + OFF_VO + (size_t)((seg * NH + h) * NT) * 1024 + lane * 16;
#pragma unroll
        for (int c = 0; c < NT; ++c) oacc[c] = mfma32(as8(*(const u32x4*)(vp + c * 1024)), as8(px), oacc[c]);
    }
    {
        u32x4 pf;
#pragma unroll
        for (int pr = 0; pr < 2; ++pr)
#pragma unroll
            for (int k = 0; k < 2; ++k) {
                auto sw = __builtin_amdgcn_permlane32_swap(pm[2 * pr][k], pm[2 * pr + 1][k], false, false);
                pf[2 * pr + k] = sw[0];
            }
        const char* up = p.ws + OFF_U + (size_t)((seg * 4 + wave) * NT) * 1024 + lane * 16;
#pragma unroll
        for (int c = 0; c < NT; ++c) oacc[c] = mfma32(as8(*(const u32x4*)(up + c * 1024)), as8(pf), oacc[c]);
    }
    __syncthreads();
#pragma unroll
    for (int c = 0; c < NT; ++c) *(f32x4*)(smem + ((wave * NT + c) * 64 + lane) * 16) = oacc[c];
    __syncthreads();
#pragma unroll
    for (int i = 0; i < 7; ++i) {
        const int c = 7 * wave + i;
        f32x4 t = *(const f32x4*)(smem + ((0 * NT + c) * 64 + lane) * 16);
#pragma unroll
        for (int w = 1; w < 4; ++w) t += *(const f32x4*)(smem + ((w * NT + c) * 64 + lane) * 16);
        *(u32x2*)(p.out + (size_t)(seg * SEG + qrow) * OUTW + 16 * c + 4 * g) = (u32x2){pk2(t[0], t[1]), pk2(t[2], t[3])};
    }
}

__global__ void __launch_bounds__(256, 1) k_b1(Params p) {
    const int tid = threadIdx.x, lane = tid & 63, wave = __builtin_amdgcn_readfirstlane(tid >> 6);
    const int hg = blockIdx.x & 3, rr = blockIdx.x >> 2;
    const int h = 4 * hg + wave, r16 = lane & 15;
    bf16x8 W[4][KS];
#pragma unroll
    for (int a = 0; a < 4; ++a)
#pragma unroll
        for (int ks = 0; ks < KS; ++ks)
            W[a][ks] = as8(*(const u32x4*)(p.ws + OFF_BW + (size_t)(((1 + (a >> 1)) * NH + h) * 2 + (a & 1)) * TILE_B + ks * 1024 + lane * 16));
    f32x4 kv[4][2];
#pragma unroll
    for (int a = 0; a < 4; ++a)
#pragma unroll
        for (int e = 0; e < 2; ++e) kv[a][e] = (f32x4){0.f, 0.f, 0.f, 0.f};
    const bool pad1 = r16 >= 12;
    for (int it = 0; it < 4; ++it) {
        f32x4 acc[2][4];
#pragma unroll
        for (int m = 0; m < 2; ++m)
#pragma unroll
            for (int a = 0; a < 4; ++a) acc[m][a] = (f32x4){0.f, 0.f, 0.f, 0.f};
        const char* xp = p.ws + OFF_BXF + (size_t)(rr * 8 + it * 2) * TILE_B + lane * 16;
#pragma unroll
        for (int ks = 0; ks < KS; ++ks)
#pragma unroll
            for (int m = 0; m < 2; ++m) {
                const bf16x8 xf = as8(*(const u32x4*)(xp + m * TILE_B + ks * 1024));
#pragma unroll
                for (int a = 0; a < 4; ++a) acc[m][a] = mfma32(xf, W[a][ks], acc[m][a]);
            }
        float ph[2][4][4];
#pragma unroll
        for (int m = 0; m < 2; ++m)
#pragma unroll
            for (int r = 0; r < 4; ++r) {
                const float p0 = acc[m][0][r], p1 = acc[m][1][r];
                const float mx = row_max(pad1 ? p0 : fmaxf(p0, p1));
                const float mn = -row_max(pad1 ? -p0 : fmaxf(-p0, -p1));
                const float a0 = __builtin_amdgcn_exp2f(p0 - mx), a1 = pad1 ? 0.f : __builtin_amdgcn_exp2f(p1 - mx);
                const float b0 = __builtin_amdgcn_exp2f(mn - p0), b1 = pad1 ? 0.f : __builtin_amdgcn_exp2f(mn - p1);
                const float ra = 1.0f / row_sum(a0 + a1), rb = 1.0f / row_sum(b0 + b1);
                ph[m][0][r] = a0 * ra; ph[m][1][r] = a1 * ra; ph[m][2][r] = b0 * rb; ph[m][3][r] = b1 * rb;
                if (r16 == 12) acc[m][3][r] = 1.0f;
            }
        u32x4 vb[2];
#pragma unroll
        for (int e = 0; e < 2; ++e)
            vb[e] = (u32x4){pk2(acc[0][2 + e][0], acc[0][2 + e][1]), pk2(acc[0][2 + e][2], acc[0][2 + e][3]), pk2(acc[1][2 + e][0], acc[1][2 + e][1]), pk2(acc[1][2 + e][2], acc[1][2 + e][3])};
#pragma unroll
        for (int a = 0; a < 4; ++a) {
            const u32x4 pa = {pk2(ph[0][a][0], ph[0][a][1]), pk2(ph[0][a][2], ph[0][a][3]), pk2(ph[1][a][0], ph[1][a][1]), pk2(ph[1][a][2], ph[1][a][3])};
#pragma unroll
            for (int e = 0; e < 2; ++e) kv[a][e] = mfma32(as8(pa), as8(vb[e]), kv[a][e]);
        }
    }
#pragma unroll
    for (int a = 0; a < 4; ++a)
#pragma unroll
        for (int e = 0; e < 2; ++e) *(f32x4*)(p.ws + OFF_BKVP + (size_t)(((rr * NH + h) * 8 + a * 2 + e) * 64 + lane) * 16) = kv[a][e];
}
__global__ void __launch_bounds__(256) k_b15(Params p) {
    const int h = blockIdx.x, tid = threadIdx.x, lane = tid & 63, w = tid >> 6;
    const int et = w >> 1, sign = w & 1;
    f32x4 lo = {0.f, 0.f, 0.f, 0.f}, hi = {0.f, 0.f, 0.f, 0.f};
#pragma unroll
    for (int rr = 0; rr < 16; ++rr) {
        lo += *(const f32x4*)(p.ws + OFF_BKVP + (size_t)(((rr * NH + h) * 8 + (2 * sign) * 2 + et) * 64 + lane) * 16);
        hi += *(const f32x4*)(p.ws + OFF_BKVP + (size_t)(((rr * NH + h) * 8 + (2 * sign + 1) * 2 + et) * 64 + lane) * 16);
    }
    *(u32x4*)(p.ws + OFF_BKV + (size_t)((h * 4 + et * 2 + sign) * 64 + lane) * 16) = (u32x4){pk2(lo[0], lo[1]), pk2(lo[2], lo[3]), pk2(hi[0], hi[1]), pk2(hi[2], hi[3])};
}
__global__ void __launch_bounds__(256, 1) k_b2(Params p) {
    extern __shared__ __attribute__((aligned(16))) char smem[];
    const int tid = threadIdx.x, lane = tid & 63, wave = __builtin_amdgcn_readfirstlane(tid >> 6);
    const int ub = blockIdx.x, r16 = lane & 15, g = lane >> 4;
    char* XL = smem;
    char* OL = smem + 28672;
    for (int i = tid; i < 2 * KS * 64; i += 256) *(u32x4*)(XL + i * 16) = *(const u32x4*)(p.ws + OFF_BXF + (size_t)ub * 28672 + (size_t)i * 16);
    __syncthreads();
    const bool padl = g == 3;
#pragma unroll 1
    for (int hl = 0; hl < 4; ++hl) {
        const int h = 4 * wave + hl;
        f32x4 acc[2][2];
#pragma unroll
        for (int m = 0; m < 2; ++m)
#pragma unroll
            for (int a = 0; a < 2; ++a) acc[m][a] = (f32x4){0.f, 0.f, 0.f, 0.f};
        const char* wp = p.ws + OFF_BW + (size_t)(h * 2) * TILE_B + lane * 16;
#pragma unroll
        for (int ks = 0; ks < KS; ++ks) {
            const bf16x8 w0 = as8(*(const u32x4*)(wp + ks * 1024)), w1 = as8(*(const u32x4*)(wp + TILE_B + ks * 1024));
#pragma unroll
            for (int m = 0; m < 2; ++m) {
                const bf16x8 xf = as8(*(const u32x4*)(XL + (m * KS + ks) * 1024 + lane * 16));
                acc[m][0] = mfma32(w0, xf, acc[m][0]);
                acc[m][1] = mfma32(w1, xf, acc[m][1]);
            }
        }
        bf16x8 kvf[2][2];
#pragma unroll
        for (int et = 0; et < 2; ++et)
#pragma unroll
            for (int sg = 0; sg < 2; ++sg) kvf[et][sg] = as8(*(const u32x4*)(p.ws + OFF_BKV + (size_t)((h * 4 + et * 2 + sg) * 64 + lane) * 16));
#pragma unroll
        for (int m = 0; m < 2; ++m) {
            float mx = -3.0e38f, mn = 3.0e38f;
#pragma unroll
            for (int r = 0; r < 4; ++r) {
                mx = fmaxf(mx, acc[m][0][r]); mn = fminf(mn, acc[m][0][r]);
                if (!padl) { mx = fmaxf(mx, acc[m][1][r]); mn = fminf(mn, acc[m][1][r]); }
            }
            mx = xg_max(mx); mn = -xg_max(-mn);
            float ea[8], eb[8], sa = 0.f, sb = 0.f;
#pragma unroll
            for (int r = 0; r < 4; ++r) {
                ea[r] = __builtin_amdgcn_exp2f(acc[m][0][r] - mx); eb[r] = __builtin_amdgcn_exp2f(mn - acc[m][0][r]);
                ea[4 + r] = padl ? 0.f : __builtin_amdgcn_exp2f(acc[m][1][r] - mx); eb[4 + r] = padl ? 0.f : __builtin_amdgcn_exp2f(mn - acc[m][1][r]);
            }
#pragma unroll
            for (int j = 0; j < 8; ++j) { sa += ea[j]; sb += eb[j]; }
            const float ra = 1.0f / xg_sum(sa), rb = 1.0f / xg_sum(sb);
            const u32x4 pa = {pk2(ea[0] * ra, ea[1] * ra), pk2(ea[2] * ra, ea[3] * ra), pk2(ea[4] * ra, ea[5] * ra), pk2(ea[6] * ra, ea[7] * ra)};
            const u32x4 pb = {pk2(eb[0] * rb, eb[1] * rb), pk2(eb[2] * rb, eb[3] * rb), pk2(eb[4] * rb, eb[5] * rb), pk2(eb[6] * rb, eb[7] * rb)};
            f32x4 nm[2];
#pragma unroll
            for (int et = 0; et < 2; ++et) {
                nm[et] = mfma32(kvf[et][0], as8(pa), (f32x4){0.f, 0.f, 0.f, 0.f});
                nm[et] = mfma32(kvf[et][1], as8(pb), nm[et]);
            }
            const float den = xg_sum(padl ? nm[1][0] : 0.f) + 1e-6f;
            const float rd = 1.0f / den;
            const u32x4 of = {pk2(nm[0][0] * rd, nm[0][1] * rd), pk2(nm[0][2] * rd, nm[0][3] * rd),
                              padl ? 0u : pk2(nm[1][0] * rd, nm[1][1] * rd), padl ? 0u : pk2(nm[1][2] * rd, nm[1][3] * rd)};
            *(u32x4*)(OL + ((m * NH + h) * 64 + lane) * 16) = of;
        }
    }
    __syncthreads();
    f32x4 oa[2][7];
#pragma unroll
    for (int m = 0; m < 2; ++m)
#pragma unroll
        for (int i = 0; i < 7; ++i) oa[m][i] = (f32x4){0.f, 0.f, 0.f, 0.f};
#pragma unroll 2
    for (int h = 0; h < NH; ++h) {
        const bf16x8 o0 = as8(*(const u32x4*)(OL + ((0 * NH + h) * 64 + lane) * 16)), o1 = as8(*(const u32x4*)(OL + ((1 * NH + h) * 64 + lane) * 16));
#pragma unroll
        for (int i = 0; i < 7; ++i) {
            const bf16x8 wf = as8(*(const u32x4*)(p.ws + OFF_BWO + (size_t)(((7 * wave + i) * NH + h) * 64 + lane) * 16));
            oa[0][i] = mfma32(wf, o0, oa[0][i]);
            oa[1][i] = mfma32(wf, o1, oa[1][i]);
        }
    }
#pragma unroll
    for (int m = 0; m < 2; ++m)
#pragma unroll
        for (int i = 0; i < 7; ++i)
            *(u32x2*)(p.out + (size_t)(ub * 32 + 16 * m + r16) * OUTW + DM + 16 * (7 * wave + i) + 4 * g) = (u32x2){pk2(oa[m][i][0], oa[m][i][1]), pk2(oa[m][i][2], oa[m][i][3])};
}

extern "C" void kernel_launch(void* const* d_in, const int* in_sizes, int n_in, void* d_out, int out_size, void* d_ws, size_t ws_size,
                              hipStream_t stream) {
    Params p;
    p.x = (const unsigned short*)d_in[0]; p.mem0 = (const unsigned short*)d_in[1]; p.Wq = (const unsigned short*)d_in[2];
    p.Wk = (const unsigned short*)d_in[3]; p.Wv = (const unsigned short*)d_in[4]; p.Wo = (const unsigned short*)d_in[5];
    p.bx = (const unsigned short*)d_in[7]; p.bwqkv = (const unsigned short*)d_in[8]; p.bwphi = (const unsigned short*)d_in[9];
    p.bwo = (const unsigned short*)d_in[10];
    p.out = (unsigned short*)d_out; p.ws = (char*)d_ws;
    const size_t a = ((size_t)d_ws + OFF_END + (2u << 20) - 1) & ~(size_t)((2u << 20) - 1);
    p.comm = (char*)a;
    hipFuncSetAttribute((const void*)k_chain, hipFuncAttributeMaxDynamicSharedMemorySize, 96 * 1024);
    hipFuncSetAttribute((const void*)k_final, hipFuncAttributeMaxDynamicSharedMemorySize, 114688);
    hipMemsetAsync(d_ws, 0, CTL_BYTES, stream);
    k0_prep<<<K0_FOLD_WGS + K0_MISC_WGS, 448, 61440, stream>>>(p);
    kg_gemm<<<168, 256, 57344, stream>>>(p);
    k_chain<<<256, 256, 96 * 1024, stream>>>(p);
    k_final<<<128, 256, 114688, stream>>>(p);
    k_b1<<<64, 256, 0, stream>>>(p);
    k_b15<<<16, 256, 0, stream>>>(p);
    k_b2<<<64, 256, 61440, stream>>>(p);
}
```

```cpp
#include <hip/hip_runtime.h>
#include <hip/hip_bf16.h>

typedef __bf16 bf16x2 __attribute__((ext_vector_type(2)));
typedef __bf16 bf16x4 __attribute__((ext_vector_type(4)));
typedef __bf16 bf16x8 __attribute__((ext_vector_type(8)));
typedef __bf16 bf16x16 __attribute__((ext_vector_type(16)));
typedef short s16x4 __attribute__((ext_vector_type(4)));
typedef float f32x4 __attribute__((ext_vector_type(4)));
typedef float f32x2 __attribute__((ext_vector_type(2)));
typedef unsigned u32x4 __attribute__((ext_vector_type(4)));
typedef unsigned u32x2 __attribute__((ext_vector_type(2)));
#define LAS __attribute__((address_space(3)))
#define GAS __attribute__((address_space(1)))
#define DI __device__ __forceinline__
#define RLX_AGENT __ATOMIC_RELAXED, __HIP_MEMORY_SCOPE_AGENT
typedef GAS unsigned gu32;

constexpr int DM = 448, NH = 16, DH = 256, SEG = 32, NSEG = 64, SEQ = 2048;
constexpr int KS = 14;
constexpr int NT = 28;
constexpr int OUTW = 896;
constexpr float LOG2E = 1.4426950408889634f;

constexpr size_t TILE_B = 14336;
constexpr size_t OFF_CTL = 0;
constexpr size_t CTL_BYTES = 8192;
constexpr size_t OFF_WB = OFF_CTL + CTL_BYTES;
constexpr size_t OFF_XF = OFF_WB + 1344 * TILE_B;
constexpr size_t OFF_KQ = OFF_XF + 128 * TILE_B;
constexpr size_t OFF_KM = OFF_KQ + 2048 * TILE_B;
constexpr size_t OFF_VO = OFF_KM + (size_t)64 * 16 * 16384;
constexpr size_t OFF_U = OFF_VO + 2048 * TILE_B;
constexpr size_t OFF_BXF = OFF_U + (size_t)64 * 4 * 28 * 1024;
constexpr size_t OFF_BW = OFF_BXF + 128 * TILE_B;
constexpr size_t OFF_BWO = OFF_BW + 96 * TILE_B;
constexpr size_t OFF_BKVP = OFF_BWO + (size_t)28 * 16 * 1024;
constexpr size_t OFF_BKV = OFF_BKVP + (size_t)16 * 16 * 8 * 1024;
constexpr size_t OFF_MH = OFF_BKV + (size_t)16 * 4 * 1024;
constexpr size_t OFF_SLAB = OFF_MH + (size_t)64 * 8192;
constexpr size_t OFF_DBG = OFF_SLAB + (size_t)512 * 28 * 1024;
constexpr size_t OFF_END = OFF_DBG + 131072;
constexpr int NUNIT = 256;
constexpr size_t COMM_BYTES = (size_t)NUNIT * 16384;
constexpr int NBLK = 4;
constexpr unsigned CPY = 10 * 16384;

constexpr int CTL_PAR = 64;
constexpr int CTL_RDY = 128, CTL_PROG = 192, CTL_DONE = 256, CTL_Q = 320, CTL_B0 = 384, CTL_B1 = 448, CTL_B15 = 512;
constexpr int CTL_TICK = 576, CTL_RDYW = 640;
constexpr int CTL_SEGC = 1024;
constexpr int CTL_VOC = 1088, CTL_TXC = 1152;
constexpr int CTL_DN = 1536;
constexpr int CTL_FT = 1280;

DI unsigned pk2(float a, float b) { bf16x2 p = {(__bf16)a, (__bf16)b}; return __builtin_bit_cast(unsigned, p); }
DI f32x4 mfma32(bf16x8 a, bf16x8 b, f32x4 c) { return __builtin_amdgcn_mfma_f32_16x16x32_bf16(a, b, c, 0, 0, 0); }
DI f32x4 mfma16(u32x2 a, u32x2 b, f32x4 c) { return __builtin_amdgcn_mfma_f32_16x16x16bf16_1k(__builtin_bit_cast(s16x4, a), __builtin_bit_cast(s16x4, b), c, 0, 0, 0); }
DI f32x4 smf(bf16x8 a, bf16x16 b, f32x4 c, int idx) { return __builtin_amdgcn_smfmac_f32_16x16x64_bf16(a, b, c, idx, 0, 0); }
DI bf16x8 as8(u32x4 v) { return __builtin_bit_cast(bf16x8, v); }
DI bf16x16 cat16(bf16x8 a, bf16x8 b) { return __builtin_shufflevector(a, b, 0, 1, 2, 3, 4, 5, 6, 7, 8, 9, 10, 11, 12, 13, 14, 15); }
DI float bf2f(unsigned short v) { return __uint_as_float((unsigned)v << 16); }
DI void lds_barrier() { asm volatile("s_waitcnt lgkmcnt(0)\n\ts_barrier" ::: "memory"); }
DI unsigned long long memtime() { unsigned long long t; asm volatile("s_memtime %0\n\ts_waitcnt lgkmcnt(0)" : "=s"(t)::"memory"); return t; }
template <int CTRL> DI float dppf(float x) { return __builtin_amdgcn_update_dpp(0.f, x, CTRL, 0xf, 0xf, true); }
template <int CTRL> DI unsigned dppu(unsigned x) { return (unsigned)__builtin_amdgcn_update_dpp(0, (int)x, CTRL, 0xf, 0xf, true); }
DI float xg_max(float x) {
    auto a = __builtin_amdgcn_permlane16_swap(__float_as_uint(x), __float_as_uint(x), false, false);
    x = fmaxf(__uint_as_float(a[0]), __uint_as_float(a[1]));
    auto b = __builtin_amdgcn_permlane32_swap(__float_as_uint(x), __float_as_uint(x), false, false);
    return fmaxf(__uint_as_float(b[0]), __uint_as_float(b[1]));
}
DI float xg_sum(float x) {
    auto a = __builtin_amdgcn_permlane16_swap(__float_as_uint(x), __float_as_uint(x), false, false);
    x = __uint_as_float(a[0]) + __uint_as_float(a[1]);
    auto b = __builtin_amdgcn_permlane32_swap(__float_as_uint(x), __float_as_uint(x), false, false);
    return __uint_as_float(b[0]) + __uint_as_float(b[1]);
}
DI float swap_add(float x, float y) {
    auto b = __builtin_amdgcn_permlane32_swap(__float_as_uint(x), __float_as_uint(y), false, false);
    return __uint_as_float(b[0]) + __uint_as_float(b[1]);
}
DI float row_max(float x) {
    x = fmaxf(x, dppf<0xB1>(x)); x = fmaxf(x, dppf<0x4E>(x)); x = fmaxf(x, dppf<0x124>(x)); x = fmaxf(x, dppf<0x128>(x)); return x;
}
DI float row_sum(float x) {
    x += dppf<0xB1>(x); x += dppf<0x4E>(x); x += dppf<0x124>(x); x += dppf<0x128>(x); return x;
}

struct Params {
    const unsigned short* x; const unsigned short* mem0; const unsigned short* Wq; const unsigned short* Wk;
    const unsigned short* Wv; const unsigned short* Wo; const unsigned short* bx; const unsigned short* bwqkv;
    const unsigned short* bwphi; const unsigned short* bwo; unsigned short* out; char* ws; char* comm;
    int d_score, d_score_s, d_score_k, d_smx, d_smm, d_outmem, d_outmem0, pad_;
};
#define DLYS 152, 0, 60, 8, 9, 3, 220
static const int k_dlys[8] = {DLYS, 10};

#define K0_AUX 16
DI void st16_wt(char* base, size_t off, u32x4 v) {
    const __amdgpu_buffer_rsrc_t r = __builtin_amdgcn_make_buffer_rsrc(base, 0, 0x7fffffff, 0x00020000);
    __builtin_amdgcn_raw_buffer_store_b128(v, r, (unsigned)off, 0, K0_AUX);
}
constexpr int K0_FOLD_WGS = 256;
constexpr int K0_MISC_WGS = 0;
constexpr int K0_THREADS = 448, K0_LDS = 61440;
#define K0T(i)
__global__ void __launch_bounds__(448) k0_prep(Params p) {
    extern __shared__ __attribute__((aligned(16))) char smem[];
    const int tid = threadIdx.x, lane = tid & 63, wave = __builtin_amdgcn_readfirstlane(tid >> 6);
    const int b = blockIdx.x;
    K0T(5)
    u32x4 xva, xvb;
    {
        const int c = b * 448 + tid, l = c & 63, ks = (c >> 6) % KS, tile = (c >> 6) / KS;
        const size_t so = (size_t)(16 * tile + (l & 15)) * DM + 32 * ks + 8 * (l >> 4);
        xva = *(const u32x4*)(p.x + so); xvb = *(const u32x4*)(p.bx + so);
    }
    {
        const int j = (b - K0_MISC_WGS) >> 3, h = 2 * (b & 7) + (j >> 4), type = (j & 8) >> 2, nq = j & 3, kh = (j >> 2) & 1;
        const unsigned short* Q = type == 0 ? p.Wk : p.Wv;
        constexpr int WO_STRIDE = 240;
        const int r16 = lane & 15, g = lane >> 4;
        u32x4 pv[8];
        if (type == 0) {
#pragma unroll
            for (int i = 0; i < 8; ++i) {
                const int c = tid + 448 * i, row = c >> 5, ch = c & 31;
                pv[i] = *(const u32x4*)(p.Wq + ((size_t)(112 * nq + row) * NH + h) * DH + ch * 8);
            }
        } else {
#pragma unroll
            for (int i = 0; i < 8; ++i) {
                const int c = tid + 448 * i, row = c / 14, ch = c % 14;
                pv[i] = *(const u32x4*)(p.Wo + ((size_t)h * DH + row) * DM + 112 * nq + ch * 8);
            }
        }
        const int ks = 7 * kh + wave;
        u32x4 qa[8][2];
#pragma unroll
        for (int es = 0; es < 8; ++es)
#pragma unroll
            for (int a = 0; a < 2; ++a) {
                const int k = 32 * ks + 8 * (r16 >> 2) + 4 * a + (r16 & 3);
                qa[es][a] = *(const u32x4*)(Q + ((size_t)k * NH + h) * DH + 8 * g + 32 * es);
            }
        if (type == 0) {
#pragma unroll
            for (int i = 0; i < 8; ++i) {
                const int c = tid + 448 * i, row = c >> 5, ch = c & 31;
                *(u32x4*)(smem + (((row >> 4) * 8 + (ch >> 2)) * 64 + (ch & 3) * 16 + (row & 15)) * 16) = pv[i];
            }
        } else {
#pragma unroll
            for (int i = 0; i < 8; ++i) {
                const int c = tid + 448 * i, row = c / 14, ch = c % 14;
                *(u32x4*)(smem + row * WO_STRIDE + ch * 16) = pv[i];
            }
        }
        {
    const int gt = b * 448 + tid, gn = K0_FOLD_WGS * 448;
    for (int c = gt; c < (int)(CTL_BYTES / 16); c += gn) *(u32x4*)(p.ws + OFF_CTL + (size_t)c * 16) = (u32x4){0u, 0u, 0u, 0u};
    st16_wt(p.ws, OFF_XF + (size_t)gt * 16, xva);
    st16_wt(p.ws, OFF_BXF + (size_t)gt * 16, xvb);
    for (int c = gt; c < 160 * 1024; c += gn) {
        u32x4 v = {0xffffffffu, 0xffffffffu, 0xffffffffu, 0xffffffffu};
        const int unit = c >> 10;
        if ((unit < 40 && unit % 10 == 0) || unit == 160) {
            const int o = c & 511;
            if (o < 448) {
                const int cs = o >> 4, ig = (o >> 3) & 1, hf = (o >> 2) & 1, i4 = o & 3;
                const unsigned* src = (const unsigned*)(p.mem0 + (size_t)(4 * ig + i4) * DM + 16 * cs + 2 * hf);
                v = (u32x4){src[0], src[2], src[4], src[6]};
#pragma unroll
                for (int j = 0; j < 4; ++j) v[j] = min(v[j], 0xfffffffeu);
            }
        }
        st16_wt(p.comm, (size_t)(unit / 40) * 1048576 + (size_t)(unit % 40) * 16384 + (size_t)(c & 1023) * 16, v);
    }
        }
        K0T(0)
        lds_barrier();
        K0T(1)
        f32x4 acc[2][7];
#pragma unroll
        for (int a = 0; a < 2; ++a)
#pragma unroll
            for (int t = 0; t < 7; ++t) acc[a][t] = (f32x4){0.f, 0.f, 0.f, 0.f};
#pragma unroll
        for (int es = 0; es < 8; ++es) {
#pragma unroll
            for (int t = 0; t < 7; ++t) {
                bf16x8 pb;
                if (type == 0) {
                    pb = as8(*(const u32x4*)(smem + ((t * 8 + es) * 64 + lane) * 16));
                } else {
                    const int q = r16 >> 2, pp = r16 & 3;
                    const char* base = smem + (32 * es + 8 * g + q) * WO_STRIDE + 32 * t + 8 * pp;
                    bf16x4 lo = __builtin_amdgcn_ds_read_tr16_b64_v4bf16((LAS bf16x4*)(base));
                    bf16x4 hi = __builtin_amdgcn_ds_read_tr16_b64_v4bf16((LAS bf16x4*)(base + 4 * WO_STRIDE));
                    pb = __builtin_shufflevector(lo, hi, 0, 1, 2, 3, 4, 5, 6, 7);
                }
#pragma unroll
                for (int a = 0; a < 2; ++a) acc[a][t] = mfma32(as8(qa[es][a]), pb, acc[a][t]);
            }
        }
        K0T(2)
        const float sc = type == 0 ? (0.0625f * LOG2E) : 1.0f;
#pragma unroll
        for (int t = 0; t < 7; ++t) {
            const int n = 112 * nq + 16 * t + r16;
            const int tile_p = 2 * (n >> 5) + ((n >> 2) & 1), row_p = 4 * ((n >> 3) & 3) + (n & 3);
            const f32x4 lo = acc[0][t], hi = acc[1][t];
            const u32x4 v = {pk2(lo[0] * sc, lo[1] * sc), pk2(lo[2] * sc, lo[3] * sc), pk2(hi[0] * sc, hi[1] * sc), pk2(hi[2] * sc, hi[3] * sc)};
            if (type == 0) st16_wt(p.ws, OFF_WB + (size_t)(h * NT + tile_p) * TILE_B + ks * 1024 + (g * 16 + row_p) * 16, v);
            else st16_wt(p.ws, OFF_WB + (size_t)((2 * NH + h) * NT + (n >> 4)) * TILE_B + ks * 1024 + lane * 16, v);
        }
        K0T(3)
    }
    K0T(4)
}

constexpr unsigned SPIN_CAP = 1u << 16;
constexpr int NSTEP = 63;
#define ASM_SCORE0 \
    "s_mov_b32 s44, %1\n" \
    "s_mov_b32 s45, %2\n" \
    "s_mov_b32 s46, %3\n" \
    "s_mov_b32 s47, %4\n" \
    "s_mov_b32 s48, %5\n" \
    "s_mov_b32 s49, %6\n" \
    "s_mov_b32 s50, %7\n" \
    "s_mov_b32 s51, %8\n" \
    "ds_read_b32 v32, %0 offset:0\n" \
    "ds_read_b32 v33, %0 offset:256\n" \
    "ds_read_b32 v34, %0 offset:512\n" \
    "ds_read_b32 v35, %0 offset:768\n" \
    "ds_read_b32 v36, %0 offset:1024\n" \
    "ds_read_b32 v37, %0 offset:1280\n" \
    "ds_read_b32 v38, %0 offset:1536\n" \
    "ds_read_b32 v39, %0 offset:1792\n" \
    "ds_read_b32 v40, %0 offset:2048\n" \
    "ds_read_b32 v41, %0 offset:2304\n" \
    "ds_read_b32 v42, %0 offset:2560\n" \
    "ds_read_b32 v43, %0 offset:2816\n" \
    "s_waitcnt lgkmcnt(0)\n" \
    "ds_read_b32 v44, %0 offset:3072\n" \
    "ds_read_b32 v45, %0 offset:3328\n" \
    "ds_read_b32 v46, %0 offset:3584\n" \
    "ds_read_b32 v47, %0 offset:3840\n" \
    "ds_read_b32 v48, %0 offset:4096\n" \
    "ds_read_b32 v49, %0 offset:4352\n" \
    "ds_read_b32 v50, %0 offset:4608\n" \
    "ds_read_b32 v51, %0 offset:4864\n" \
    "ds_read_b32 v52, %0 offset:5120\n" \
    "ds_read_b32 v53, %0 offset:5376\n" \
    "ds_read_b32 v54, %0 offset:5632\n" \
    "ds_read_b32 v55, %0 offset:5888\n" \
    "s_waitcnt lgkmcnt(0)\n" \
    "ds_read_b32 v56, %0 offset:6144\n" \
    "ds_read_b32 v57, %0 offset:6400\n" \
    "ds_read_b32 v58, %0 offset:6656\n" \
    "ds_read_b32 v59, %0 offset:6912\n" \
    "ds_read_b32 v60, %0 offset:7168\n" \
    "ds_read_b32 v61, %0 offset:7424\n" \
    "s_mov_b32 s41, 0\n" \
    "s_mov_b32 s40, 0\n" \
    "s_mov_b32 s69, %10\n" \
    "s_mov_b32 s71, %11\n" \
    "s_mov_b32 s56, %12\n" \
    "s_mov_b32 s63, 0\n" \
    "s_mov_b32 s52, 0\n" \
    "s_mov_b32 s65, 0\n" \
    "s_mov_b32 s66, 0\n" \
    "s_mov_b32 s67, 0\n" \
    "s_waitcnt lgkmcnt(0)\n" \
    "s_mov_b32 s59, %15\n" \
    "s_mov_b32 s60, %17\n" \
    "s_mov_b32 s58, %13\n" \
    "s_lshl_b32 s55, s58, 11\n" \
    "s_add_u32 s64, s55, %14\n" \
    "buffer_load_dwordx4 a[0:3], v49, s[48:51], s64 offen\n" \
    "buffer_load_dwordx4 a[4:7], v49, s[48:51], s64 offen offset:1024\n" \
    "s_add_u32 s64, s64, 14336\n" \
    "buffer_load_dwordx4 a[56:59], v49, s[48:51], s64 offen\n" \
    "buffer_load_dwordx4 a[60:63], v49, s[48:51], s64 offen offset:1024\n" \
    "s_add_u32 s58, s58, 1\n" \
    "s_cmp_eq_u32 s58, 7\n" \
    "s_cselect_b32 s58, 0, s58\n" \
    "s_lshl_b32 s55, s58, 11\n" \
    "s_add_u32 s64, s55, %14\n" \
    "buffer_load_dwordx4 a[8:11], v49, s[48:51], s64 offen\n" \
    "buffer_load_dwordx4 a[12:15], v49, s[48:51], s64 offen offset:1024\n" \
    "s_add_u32 s64, s64, 14336\n" \
    "buffer_load_dwordx4 a[64:67], v49, s[48:51], s64 offen\n" \
    "buffer_load_dwordx4 a[68:71], v49, s[48:51], s64 offen offset:1024\n" \
    "s_add_u32 s58, s58, 1\n" \
    "s_cmp_eq_u32 s58, 7\n" \
    "s_cselect_b32 s58, 0, s58\n" \
    "s_lshl_b32 s55, s58, 11\n" \
    "s_add_u32 s64, s55, %14\n" \
    "buffer_load_dwordx4 a[16:19], v49, s[48:51], s64 offen\n" \
    "buffer_load_dwordx4 a[20:23], v49, s[48:51], s64 offen offset:1024\n" \
    "s_add_u32 s64, s64, 14336\n" \
    "buffer_load_dwordx4 a[72:75], v49, s[48:51], s64 offen\n" \
    "buffer_load_dwordx4 a[76:79], v49, s[48:51], s64 offen offset:1024\n" \
    "s_add_u32 s58, s58, 1\n" \
    "s_cmp_eq_u32 s58, 7\n" \
    "s_cselect_b32 s58, 0, s58\n" \
    "s_lshl_b32 s55, s58, 11\n" \
    "s_add_u32 s64, s55, %14\n" \
    "buffer_load_dwordx4 a[24:27], v49, s[48:51], s64 offen\n" \
    "buffer_load_dwordx4 a[28:31], v49, s[48:51], s64 offen offset:1024\n" \
    "s_add_u32 s64, s64, 14336\n" \
    "buffer_load_dwordx4 a[80:83], v49, s[48:51], s64 offen\n" \
    "buffer_load_dwordx4 a[84:87], v49, s[48:51], s64 offen offset:1024\n" \
    "s_add_u32 s58, s58, 1\n" \
    "s_cmp_eq_u32 s58, 7\n" \
    "s_cselect_b32 s58, 0, s58\n" \
    "s_lshl_b32 s55, s58, 11\n" \
    "s_add_u32 s64, s55, %14\n" \
    "buffer_load_dwordx4 a[32:35], v49, s[48:51], s64 offen\n" \
    "buffer_load_dwordx4 a[36:39], v49, s[48:51], s64 offen offset:1024\n" \
    "s_add_u32 s64, s64, 14336\n" \
    "buffer_load_dwordx4 a[88:91], v49, s[48:51], s64 offen\n" \
    "buffer_load_dwordx4 a[92:95], v49, s[48:51], s64 offen offset:1024\n" \
    "s_add_u32 s58, s58, 1\n" \
    "s_cmp_eq_u32 s58, 7\n" \
    "s_cselect_b32 s58, 0, s58\n" \
    "s_lshl_b32 s55, s58, 11\n" \
    "s_add_u32 s64, s55, %14\n" \
    "buffer_load_dwordx4 a[40:43], v49, s[48:51], s64 offen\n" \
    "buffer_load_dwordx4 a[44:47], v49, s[48:51], s64 offen offset:1024\n" \
    "s_add_u32 s64, s64, 14336\n" \
    "buffer_load_dwordx4 a[96:99], v49, s[48:51], s64 offen\n" \
    "buffer_load_dwordx4 a[100:103], v49, s[48:51], s64 offen offset:1024\n" \
    "s_add_u32 s58, s58, 1\n" \
    "s_cmp_eq_u32 s58, 7\n" \
    "s_cselect_b32 s58, 0, s58\n" \
    "s_lshl_b32 s55, s58, 11\n" \
    "s_add_u32 s64, s55, %14\n" \
    "buffer_load_dwordx4 a[48:51], v49, s[48:51], s64 offen\n" \
    "buffer_load_dwordx4 a[52:55], v49, s[48:51], s64 offen offset:1024\n" \
    "s_add_u32 s64, s64, 14336\n" \
    "buffer_load_dwordx4 a[104:107], v49, s[48:51], s64 offen\n" \
    "buffer_load_dwordx4 a[108:111], v49, s[48:51], s64 offen offset:1024\n" \
    "s_add_u32 s58, s58, 1\n" \
    "s_cmp_eq_u32 s58, 7\n" \
    "s_cselect_b32 s58, 0, s58\n" \
    "s_cmp_lt_u32 s40, s41\n" \
    "s_cbranch_scc1 LS0_F0OK%=\n" \
    "LS0_F0P%=:\n" \
    "buffer_load_dword v138, v52, s[48:51], 0 offen offset:3328 sc1\n" \
    "s_waitcnt vmcnt(0)\n" \
    "v_cmp_gt_u32 vcc, 28, v138\n" \
    "s_ff1_i32_b64 s41, vcc\n" \
    "s_cmp_lt_u32 s40, s41\n" \
    "s_cbranch_scc1 LS0_F0OK%=\n" \
    "s_add_u32 s67, s67, 1\n" \
    "s_cmp_gt_u32 s67, 65536\n" \
    "s_cbranch_scc1 LS0_F0OK%=\n" \
    "s_sleep 4\n" \
    "s_branch LS0_F0P%=\n" \
    "LS0_F0OK%=:\n" \
    "buffer_load_dwordx2 v[156:157], v56, s[48:51], s59 offen offset:0 sc1\n" \
    "buffer_load_dwordx2 v[160:161], v56, s[48:51], s59 offen offset:1024 sc1\n" \
    "buffer_load_dwordx2 v[164:165], v56, s[48:51], s59 offen offset:2048 sc1\n" \
    "buffer_load_dwordx2 v[168:169], v56, s[48:51], s59 offen offset:3072 sc1\n" \
    "s_add_u32 s64, s59, 4096\n" \
    "buffer_load_dwordx2 v[172:173], v56, s[48:51], s64 offen sc1\n" \
    "s_waitcnt vmcnt(0)\n" \
    "s_barrier\n" \
    "v_mov_b32 v64, 1\n" \
    "buffer_atomic_add v64, v53, s[48:51], 0 offen sc0\n" \
    "s_waitcnt vmcnt(0)\n" \
    "v_readfirstlane_b32 s55, v64\n" \
    "s_cmp_lg_u32 s55, 119\n" \
    "s_cbranch_scc1 LS0_HSW%=\n" \
    "s_memrealtime s[72:73]\n" \
    "s_waitcnt lgkmcnt(0)\n" \
    "s_add_u32 s55, s72, %9\n" \
    "s_andn2_b32 s55, s55, 3\n" \
    "s_add_u32 s64, s55, s56\n" \
    "v_mov_b32 v68, s64\n" \
    "v_mov_b32 v69, 0\n" \
    "v_subrev_u32 v65, 1024, v52\n" \
    "v_lshrrev_b32 v66, 3, v65\n" \
    "v_mul_u32_u24 v66, 163840, v66\n" \
    "v_and_b32 v67, 4, v65\n" \
    "v_lshlrev_b32 v67, 11, v67\n" \
    "v_add3_u32 v66, v66, v67, v67\n" \
    "v_sub_u32 v66, v66, v67\n" \
    "v_add_u32 v66, 7168, v66\n" \
    "v_mov_b32 v67, 0x7fffff00\n" \
    "v_cmp_gt_u32 vcc, 32, v65\n" \
    "s_nop 1\n" \
    "v_cndmask_b32 v66, v67, v66, vcc\n" \
    "buffer_store_dwordx2 v[68:69], v66, s[44:47], 0 offen sc1\n" \
    "s_or_b32 s55, s55, 1\n" \
    "v_mov_b32 v65, s55\n" \
    "s_waitcnt vmcnt(0)\n" \
    "v_subrev_u32 v66, 240, v53\n" \
    "buffer_store_dword v65, v66, s[48:51], 0 offen sc1\n" \
    "LS0_HSW%=:\n" \
    "v_subrev_u32 v65, 768, v52\n" \
    "LS0_HSP%=:\n" \
    "buffer_load_dword v64, v65, s[48:51], 0 offen sc1\n" \
    "s_waitcnt vmcnt(0)\n" \
    "v_readlane_b32 s68, v64, 4\n" \
    "v_cmp_eq_u32 vcc, 0, v64\n" \
    "s_and_b32 s55, vcc_lo, 31\n" \
    "s_cmp_eq_u32 s55, 0\n" \
    "s_cbranch_scc1 LS0_HSD%=\n" \
    "s_add_u32 s67, s67, 1\n" \
    "s_cmp_gt_u32 s67, 65536\n" \
    "s_cbranch_scc1 LS0_HSD%=\n" \
    "s_sleep 2\n" \
    "s_branch LS0_HSP%=\n" \
    "LS0_HSD%=:\n" \
    "s_andn2_b32 s68, s68, 3\n" \
    "v_and_b32 v64, 2, v64\n" \
    "v_lshlrev_b32 v64, 12, v64\n" \
    "v_subrev_u32 v48, 1024, v52\n" \
    "v_lshlrev_b32 v48, 18, v48\n" \
    "v_add_u32 v48, v48, v64\n" \
    "s_mov_b32 s67, 0\n" \
    "s_nop 0\n" \
    "v_readlane_b32 s53, v48, 0\n" \
    "v_readlane_b32 s43, v48, 2\n" \
    "v_mov_b32 v152, -1\n" \
    "v_mov_b32 v153, -1\n" \
    "v_mov_b32 v154, -1\n" \
    "v_mov_b32 v155, -1\n" \
    "v_mov_b64 v[158:159], 0\n" \
    "v_mov_b64 v[162:163], 0\n" \
    "v_mov_b64 v[166:167], 0\n" \
    "v_mov_b64 v[170:171], 0\n" \
    "v_mov_b64 v[174:175], 0\n" \
    "v_mov_b64 v[178:179], 0\n" \
    "v_mov_b32 v176, 0x3f803f80\n" \
    "v_mov_b32 v177, 0x3f803f80\n" \
    "v_mov_b64 v[182:183], 0\n" \
    "LS0_LOOP%=:\n" \
    "s_and_b32 s70, s68, 3\n" \
    "s_andn2_b32 s64, s68, 3\n" \
    "LS0_CWC%=:\n" \
    "s_memrealtime s[72:73]\n" \
    "s_waitcnt lgkmcnt(0)\n" \
    "s_sub_u32 s55, s72, s64\n" \
    "s_cmp_ge_i32 s55, -12\n" \
    "s_cbranch_scc1 LS0_CW%=\n" \
    "s_cmp_lt_i32 s55, -4096\n" \
    "s_cbranch_scc1 LS0_CWO%=\n" \
    "s_sleep 2\n" \
    "s_branch LS0_CWC%=\n" \
    "LS0_CW%=:\n" \
    "s_memrealtime s[72:73]\n" \
    "s_waitcnt lgkmcnt(0)\n" \
    "s_sub_u32 s55, s72, s64\n" \
    "s_cmp_ge_i32 s55, 0\n" \
    "s_cbranch_scc1 LS0_CWO%=\n" \
    "s_memrealtime s[72:73]\n" \
    "s_waitcnt lgkmcnt(0)\n" \
    "s_sub_u32 s55, s72, s64\n" \
    "s_cmp_ge_i32 s55, 0\n" \
    "s_cbranch_scc1 LS0_CWO%=\n" \
    "s_memrealtime s[72:73]\n" \
    "s_waitcnt lgkmcnt(0)\n" \
    "s_sub_u32 s55, s72, s64\n" \
    "s_cmp_ge_i32 s55, 0\n" \
    "s_cbranch_scc1 LS0_CWO%=\n" \
    "s_memrealtime s[72:73]\n" \
    "s_waitcnt lgkmcnt(0)\n" \
    "s_sub_u32 s55, s72, s64\n" \
    "s_cmp_ge_i32 s55, 0\n" \
    "s_cbranch_scc1 LS0_CWO%=\n" \
    "s_branch LS0_CW%=\n" \
    "LS0_CWO%=:\n" \
    "s_mov_b32 s58, s70\n" \
    "LS0_CWD%=:\n" \
    "s_sub_u32 s58, s58, 1\n" \
    "s_cbranch_scc0 LS0_CWD%=\n" \
    "LS0_RETRY%=:\n" \
    "buffer_load_dwordx4 v[64:67], v32, s[44:47], s53 offen sc1\n" \
    "buffer_load_dwordx4 v[68:71], v33, s[44:47], s53 offen sc1\n" \
    "v_mov_b64 v[100:101], 0\n" \
    "v_mov_b64 v[104:105], 0\n" \
    "v_mov_b64 v[102:103], 0\n" \
    "v_mov_b64 v[106:107], 0\n" \
    "s_waitcnt vmcnt(1)\n" \
    "ds_write_b128 v34, v[64:67]\n" \
    "v_smfmac_f32_16x16x64_bf16 v[104:107], v[64:67], a[56:63], v47\n" \
    "v_max3_u32 v136, v64, v65, v66\n" \
    "s_waitcnt vmcnt(0)\n" \
    "ds_write_b128 v35, v[68:71]\n" \
    "v_smfmac_f32_16x16x64_bf16 v[104:107], v[68:71], a[64:71], v47\n" \
    "v_max3_u32 v136, v136, v67, v68\n" \
    "v_max3_u32 v136, v136, v69, v70\n" \
    "v_smfmac_f32_16x16x64_bf16 v[100:103], v[64:67], a[0:7], v47\n" \
    "v_max_u32 v136, v136, v71\n" \
    "v_cmp_eq_u32 vcc, -1, v136\n" \
    "v_smfmac_f32_16x16x64_bf16 v[100:103], v[68:71], a[8:15], v47\n" \
    "s_cbranch_vccnz LS0_SLOW%=\n" \
    "LS0_CONT%=:\n" \
    "s_waitcnt lgkmcnt(0)\n" \
    "s_barrier\n" \
    "ds_read_b128 v[72:75], v36\n" \
    "ds_read_b128 v[76:79], v37\n" \
    "ds_read_b128 v[80:83], v38\n" \
    "ds_read_b128 v[84:87], v39\n" \
    "ds_read_b128 v[88:91], v40\n" \
    "ds_read_b64 v[150:151], v51 offset:7168\n" \
    "s_waitcnt lgkmcnt(5)\n" \
    "v_smfmac_f32_16x16x64_bf16 v[104:107], v[72:75], a[72:79], v47\n" \
    "v_smfmac_f32_16x16x64_bf16 v[100:103], v[72:75], a[16:23], v47\n" \
    "s_waitcnt lgkmcnt(4)\n" \
    "v_smfmac_f32_16x16x64_bf16 v[104:107], v[76:79], a[80:87], v47\n" \
    "v_smfmac_f32_16x16x64_bf16 v[100:103], v[76:79], a[24:31], v47\n" \
    "s_waitcnt lgkmcnt(3)\n" \
    "v_smfmac_f32_16x16x64_bf16 v[104:107], v[80:83], a[88:95], v47\n" \
    "s_waitcnt lgkmcnt(2)\n" \
    "v_smfmac_f32_16x16x64_bf16 v[104:107], v[84:87], a[96:103], v47\n" \
    "s_waitcnt lgkmcnt(1)\n" \
    "v_smfmac_f32_16x16x64_bf16 v[104:107], v[88:91], a[104:111], v47\n" \
    "v_smfmac_f32_16x16x64_bf16 v[100:103], v[80:83], a[32:39], v47\n" \
    "s_nop 2\n" \
    "v_smfmac_f32_16x16x64_bf16 v[100:103], v[84:87], a[40:47], v47\n" \
    "s_nop 2\n" \
    "v_cvt_pk_bf16_f32 v138, v104, v105\n" \
    "v_smfmac_f32_16x16x64_bf16 v[100:103], v[88:91], a[48:55], v47\n" \
    "v_cvt_pk_bf16_f32 v139, v106, v107\n" \
    "ds_write_b64 v44, v[138:139]\n" \
    "s_nop 5\n" \
    "v_cvt_pk_bf16_f32 v140, v100, v101\n" \
    "v_cvt_pk_bf16_f32 v141, v102, v103\n" \
    "ds_write_b64 v43, v[140:141]\n" \
    "s_waitcnt lgkmcnt(0)\n" \
    "s_barrier\n" \
    "s_cmp_eq_u32 s52, 63\n" \
    "s_cbranch_scc1 LS0_END%=\n" \
    "ds_read_b64 v[180:181], v54\n" \
    "s_waitcnt lgkmcnt(0)\n" \
    "v_mfma_f32_16x16x32_bf16 v[112:115], v[180:183], v[156:159], 0\n" \
    "v_mfma_f32_16x16x32_bf16 v[116:119], v[180:183], v[160:163], 0\n" \
    "v_mfma_f32_16x16x32_bf16 v[120:123], v[180:183], v[164:167], 0\n" \
    "v_mfma_f32_16x16x32_bf16 v[124:127], v[180:183], v[168:171], 0\n" \
    "v_mfma_f32_16x16x32_bf16 v[128:131], v[180:183], v[172:175], 0\n" \
    "s_nop 3\n" \
    "v_cvt_pk_bf16_f32 v112, v112, v113\n" \
    "v_cvt_pk_bf16_f32 v113, v114, v115\n" \
    "v_cvt_pk_bf16_f32 v116, v116, v117\n" \
    "v_cvt_pk_bf16_f32 v117, v118, v119\n" \
    "s_nop 0\n" \
    "v_permlane32_swap_b32 v112, v116\n" \
    "v_permlane32_swap_b32 v113, v117\n" \
    "v_min_u32 v112, -2, v112\n" \
    "v_min_u32 v113, -2, v113\n" \
    "buffer_store_dwordx2 v[112:113], v57, s[44:47], s53 offen sc1\n" \
    "v_cvt_pk_bf16_f32 v120, v120, v121\n" \
    "v_cvt_pk_bf16_f32 v121, v122, v123\n" \
    "v_cvt_pk_bf16_f32 v124, v124, v125\n" \
    "v_cvt_pk_bf16_f32 v125, v126, v127\n" \
    "s_nop 0\n" \
    "v_permlane32_swap_b32 v120, v124\n" \
    "v_permlane32_swap_b32 v121, v125\n" \
    "v_min_u32 v120, -2, v120\n" \
    "v_min_u32 v121, -2, v121\n" \
    "buffer_store_dwordx2 v[120:121], v58, s[44:47], s53 offen sc1\n" \
    "v_cvt_pk_bf16_f32 v128, v128, v129\n" \
    "v_cvt_pk_bf16_f32 v129, v130, v131\n" \
    "v_min_u32 v128, -2, v128\n" \
    "v_min_u32 v129, -2, v129\n" \
    "buffer_store_dwordx2 v[128:129], v59, s[44:47], s53 offen sc1\n" \
    "v_readfirstlane_b32 s55, v151\n" \
    "s_max_u32 s41, s41, s55\n" \
    "s_add_u32 s52, s52, 1\n" \
    "s_cmp_lt_u32 s52, 63\n" \
    "s_cselect_b32 s55, 458752, 0\n" \
    "s_add_u32 s59, s59, s55\n" \
    "s_min_u32 s40, s52, 62\n" \
    "s_cmp_lt_u32 s40, s41\n" \
    "s_cbranch_scc1 LS0_F1OK%=\n" \
    "LS0_F1P%=:\n" \
    "buffer_load_dword v138, v52, s[48:51], 0 offen offset:3328 sc1\n" \
    "s_waitcnt vmcnt(0)\n" \
    "v_cmp_gt_u32 vcc, 28, v138\n" \
    "s_ff1_i32_b64 s41, vcc\n" \
    "s_cmp_lt_u32 s40, s41\n" \
    "s_cbranch_scc1 LS0_F1OK%=\n" \
    "s_add_u32 s67, s67, 1\n" \
    "s_cmp_gt_u32 s67, 65536\n" \
    "s_cbranch_scc1 LS0_F1OK%=\n" \
    "s_sleep 4\n" \
    "s_branch LS0_F1P%=\n" \
    "LS0_F1OK%=:\n" \
    "buffer_load_dwordx2 v[156:157], v56, s[48:51], s59 offen offset:0 sc1\n" \
    "buffer_load_dwordx2 v[160:161], v56, s[48:51], s59 offen offset:1024 sc1\n" \
    "buffer_load_dwordx2 v[164:165], v56, s[48:51], s59 offen offset:2048 sc1\n" \
    "buffer_load_dwordx2 v[168:169], v56, s[48:51], s59 offen offset:3072 sc1\n" \
    "s_add_u32 s64, s59, 4096\n" \
    "buffer_load_dwordx2 v[172:173], v56, s[48:51], s64 offen sc1\n" \
    "s_mov_b32 s58, %16\n" \
    "LS0_WS%=:\n" \
    "s_sleep 1\n" \
    "s_sub_u32 s58, s58, 1\n" \
    "s_cbranch_scc0 LS0_WS%=\n" \
    "buffer_store_dwordx2 v[152:153], v57, s[44:47], s43 offen sc1\n" \
    "buffer_store_dwordx2 v[152:153], v58, s[44:47], s43 offen sc1\n" \
    "buffer_store_dwordx2 v[152:153], v59, s[44:47], s43 offen sc1\n" \
    "s_and_b32 s55, s52, 3\n" \
    "s_xor_b32 s64, s55, 2\n" \
    "s_nop 1\n" \
    "v_readlane_b32 s53, v48, s55\n" \
    "v_readlane_b32 s43, v48, s64\n" \
    "v_readfirstlane_b32 s68, v150\n" \
    "s_branch LS0_LOOP%=\n" \
    "LS0_SLOW%=:\n" \
    "s_add_u32 s65, s65, 1\n" \
    "s_cmp_gt_u32 s65, 65536\n" \
    "s_cbranch_scc1 LS0_CONT%=\n" \
    "s_mov_b32 s63, 1\n" \
    "s_nop 7\n" \
    "s_branch LS0_RETRY%=\n" \
    "LS0_END%=:\n" \
    "s_waitcnt vmcnt(0)\n"
#define ASM_SCORE2 \
    "s_mov_b32 s44, %1\n" \
    "s_mov_b32 s45, %2\n" \
    "s_mov_b32 s46, %3\n" \
    "s_mov_b32 s47, %4\n" \
    "s_mov_b32 s48, %5\n" \
    "s_mov_b32 s49, %6\n" \
    "s_mov_b32 s50, %7\n" \
    "s_mov_b32 s51, %8\n" \
    "ds_read_b32 v32, %0 offset:0\n" \
    "ds_read_b32 v33, %0 offset:256\n" \
    "ds_read_b32 v34, %0 offset:512\n" \
    "ds_read_b32 v35, %0 offset:768\n" \
    "ds_read_b32 v36, %0 offset:1024\n" \
    "ds_read_b32 v37, %0 offset:1280\n" \
    "ds_read_b32 v38, %0 offset:1536\n" \
    "ds_read_b32 v39, %0 offset:1792\n" \
    "ds_read_b32 v40, %0 offset:2048\n" \
    "ds_read_b32 v41, %0 offset:2304\n" \
    "ds_read_b32 v42, %0 offset:2560\n" \
    "ds_read_b32 v43, %0 offset:2816\n" \
    "s_waitcnt lgkmcnt(0)\n" \
    "ds_read_b32 v44, %0 offset:3072\n" \
    "ds_read_b32 v45, %0 offset:3328\n" \
    "ds_read_b32 v46, %0 offset:3584\n" \
    "ds_read_b32 v47, %0 offset:3840\n" \
    "ds_read_b32 v48, %0 offset:4096\n" \
    "ds_read_b32 v49, %0 offset:4352\n" \
    "ds_read_b32 v50, %0 offset:4608\n" \
    "ds_read_b32 v51, %0 offset:4864\n" \
    "ds_read_b32 v52, %0 offset:5120\n" \
    "ds_read_b32 v53, %0 offset:5376\n" \
    "ds_read_b32 v54, %0 offset:5632\n" \
    "ds_read_b32 v55, %0 offset:5888\n" \
    "s_waitcnt lgkmcnt(0)\n" \
    "ds_read_b32 v56, %0 offset:6144\n" \
    "ds_read_b32 v57, %0 offset:6400\n" \
    "ds_read_b32 v58, %0 offset:6656\n" \
    "ds_read_b32 v59, %0 offset:6912\n" \
    "ds_read_b32 v60, %0 offset:7168\n" \
    "ds_read_b32 v61, %0 offset:7424\n" \
    "s_mov_b32 s41, 0\n" \
    "s_mov_b32 s40, 0\n" \
    "s_mov_b32 s69, %10\n" \
    "s_mov_b32 s71, %11\n" \
    "s_mov_b32 s56, %12\n" \
    "s_mov_b32 s63, 0\n" \
    "s_mov_b32 s52, 0\n" \
    "s_mov_b32 s65, 0\n" \
    "s_mov_b32 s66, 0\n" \
    "s_mov_b32 s67, 0\n" \
    "s_waitcnt lgkmcnt(0)\n" \
    "s_mov_b32 s59, %15\n" \
    "s_mov_b32 s60, %17\n" \
    "s_mov_b32 s58, %13\n" \
    "s_lshl_b32 s55, s58, 11\n" \
    "s_add_u32 s64, s55, %14\n" \
    "buffer_load_dwordx4 a[0:3], v49, s[48:51], s64 offen\n" \
    "buffer_load_dwordx4 a[4:7], v49, s[48:51], s64 offen offset:1024\n" \
    "s_add_u32 s64, s64, 14336\n" \
    "buffer_load_dwordx4 a[56:59], v49, s[48:51], s64 offen\n" \
    "buffer_load_dwordx4 a[60:63], v49, s[48:51], s64 offen offset:1024\n" \
    "s_add_u32 s58, s58, 1\n" \
    "s_cmp_eq_u32 s58, 7\n" \
    "s_cselect_b32 s58, 0, s58\n" \
    "s_lshl_b32 s55, s58, 11\n" \
    "s_add_u32 s64, s55, %14\n" \
    "buffer_load_dwordx4 a[8:11], v49, s[48:51], s64 offen\n" \
    "buffer_load_dwordx4 a[12:15], v49, s[48:51], s64 offen offset:1024\n" \
    "s_add_u32 s64, s64, 14336\n" \
    "buffer_load_dwordx4 a[64:67], v49, s[48:51], s64 offen\n" \
    "buffer_load_dwordx4 a[68:71], v49, s[48:51], s64 offen offset:1024\n" \
    "s_add_u32 s58, s58, 1\n" \
    "s_cmp_eq_u32 s58, 7\n" \
    "s_cselect_b32 s58, 0, s58\n" \
    "s_lshl_b32 s55, s58, 11\n" \
    "s_add_u32 s64, s55, %14\n" \
    "buffer_load_dwordx4 a[16:19], v49, s[48:51], s64 offen\n" \
    "buffer_load_dwordx4 a[20:23], v49, s[48:51], s64 offen offset:1024\n" \
    "s_add_u32 s64, s64, 14336\n" \
    "buffer_load_dwordx4 a[72:75], v49, s[48:51], s64 offen\n" \
    "buffer_load_dwordx4 a[76:79], v49, s[48:51], s64 offen offset:1024\n" \
    "s_add_u32 s58, s58, 1\n" \
    "s_cmp_eq_u32 s58, 7\n" \
    "s_cselect_b32 s58, 0, s58\n" \
    "s_lshl_b32 s55, s58, 11\n" \
    "s_add_u32 s64, s55, %14\n" \
    "buffer_load_dwordx4 a[24:27], v49, s[48:51], s64 offen\n" \
    "buffer_load_dwordx4 a[28:31], v49, s[48:51], s64 offen offset:1024\n" \
    "s_add_u32 s64, s64, 14336\n" \
    "buffer_load_dwordx4 a[80:83], v49, s[48:51], s64 offen\n" \
    "buffer_load_dwordx4 a[84:87], v49, s[48:51], s64 offen offset:1024\n" \
    "s_add_u32 s58, s58, 1\n" \
    "s_cmp_eq_u32 s58, 7\n" \
    "s_cselect_b32 s58, 0, s58\n" \
    "s_lshl_b32 s55, s58, 11\n" \
    "s_add_u32 s64, s55, %14\n" \
    "buffer_load_dwordx4 a[32:35], v49, s[48:51], s64 offen\n" \
    "buffer_load_dwordx4 a[36:39], v49, s[48:51], s64 offen offset:1024\n" \
    "s_add_u32 s64, s64, 14336\n" \
    "buffer_load_dwordx4 a[88:91], v49, s[48:51], s64 offen\n" \
    "buffer_load_dwordx4 a[92:95], v49, s[48:51], s64 offen offset:1024\n" \
    "s_add_u32 s58, s58, 1\n" \
    "s_cmp_eq_u32 s58, 7\n" \
    "s_cselect_b32 s58, 0, s58\n" \
    "s_lshl_b32 s55, s58, 11\n" \
    "s_add_u32 s64, s55, %14\n" \
    "buffer_load_dwordx4 a[40:43], v49, s[48:51], s64 offen\n" \
    "buffer_load_dwordx4 a[44:47], v49, s[48:51], s64 offen offset:1024\n" \
    "s_add_u32 s64, s64, 14336\n" \
    "buffer_load_dwordx4 a[96:99], v49, s[48:51], s64 offen\n" \
    "buffer_load_dwordx4 a[100:103], v49, s[48:51], s64 offen offset:1024\n" \
    "s_add_u32 s58, s58, 1\n" \
    "s_cmp_eq_u32 s58, 7\n" \
    "s_cselect_b32 s58, 0, s58\n" \
    "s_lshl_b32 s55, s58, 11\n" \
    "s_add_u32 s64, s55, %14\n" \
    "buffer_load_dwordx4 a[48:51], v49, s[48:51], s64 offen\n" \
    "buffer_load_dwordx4 a[52:55], v49, s[48:51], s64 offen offset:1024\n" \
    "s_add_u32 s64, s64, 14336\n" \
    "buffer_load_dwordx4 a[104:107], v49, s[48:51], s64 offen\n" \
    "buffer_load_dwordx4 a[108:111], v49, s[48:51], s64 offen offset:1024\n" \
    "s_add_u32 s58, s58, 1\n" \
    "s_cmp_eq_u32 s58, 7\n" \
    "s_cselect_b32 s58, 0, s58\n" \
    "s_cmp_lt_u32 s40, s41\n" \
    "s_cbranch_scc1 LS2_F0OK%=\n" \
    "LS2_F0P%=:\n" \
    "buffer_load_dword v138, v52, s[48:51], 0 offen offset:3328 sc1\n" \
    "s_waitcnt vmcnt(0)\n" \
    "v_cmp_gt_u32 vcc, 28, v138\n" \
    "s_ff1_i32_b64 s41, vcc\n" \
    "s_cmp_lt_u32 s40, s41\n" \
    "s_cbranch_scc1 LS2_F0OK%=\n" \
    "s_add_u32 s67, s67, 1\n" \
    "s_cmp_gt_u32 s67, 65536\n" \
    "s_cbranch_scc1 LS2_F0OK%=\n" \
    "s_sleep 4\n" \
    "s_branch LS2_F0P%=\n" \
    "LS2_F0OK%=:\n" \
    "buffer_load_dwordx2 v[156:157], v56, s[48:51], s59 offen offset:0 sc1\n" \
    "buffer_load_dwordx2 v[160:161], v56, s[48:51], s59 offen offset:1024 sc1\n" \
    "buffer_load_dwordx2 v[164:165], v56, s[48:51], s59 offen offset:2048 sc1\n" \
    "buffer_load_dwordx2 v[168:169], v56, s[48:51], s59 offen offset:3072 sc1\n" \
    "s_add_u32 s64, s59, 4096\n" \
    "buffer_load_dwordx2 v[172:173], v56, s[48:51], s64 offen sc1\n" \
    "s_waitcnt vmcnt(0)\n" \
    "s_barrier\n" \
    "v_mov_b32 v64, 1\n" \
    "buffer_atomic_add v64, v53, s[48:51], 0 offen sc0\n" \
    "s_waitcnt vmcnt(0)\n" \
    "v_readfirstlane_b32 s55, v64\n" \
    "s_cmp_lg_u32 s55, 119\n" \
    "s_cbranch_scc1 LS2_HSW%=\n" \
    "s_memrealtime s[72:73]\n" \
    "s_waitcnt lgkmcnt(0)\n" \
    "s_add_u32 s55, s72, %9\n" \
    "s_andn2_b32 s55, s55, 3\n" \
    "s_add_u32 s64, s55, s56\n" \
    "v_mov_b32 v68, s64\n" \
    "v_mov_b32 v69, 0\n" \
    "v_subrev_u32 v65, 1024, v52\n" \
    "v_lshrrev_b32 v66, 3, v65\n" \
    "v_mul_u32_u24 v66, 163840, v66\n" \
    "v_and_b32 v67, 4, v65\n" \
    "v_lshlrev_b32 v67, 11, v67\n" \
    "v_add3_u32 v66, v66, v67, v67\n" \
    "v_sub_u32 v66, v66, v67\n" \
    "v_add_u32 v66, 7168, v66\n" \
    "v_mov_b32 v67, 0x7fffff00\n" \
    "v_cmp_gt_u32 vcc, 32, v65\n" \
    "s_nop 1\n" \
    "v_cndmask_b32 v66, v67, v66, vcc\n" \
    "buffer_store_dwordx2 v[68:69], v66, s[44:47], 0 offen sc1\n" \
    "s_or_b32 s55, s55, 1\n" \
    "v_mov_b32 v65, s55\n" \
    "s_waitcnt vmcnt(0)\n" \
    "v_subrev_u32 v66, 240, v53\n" \
    "buffer_store_dword v65, v66, s[48:51], 0 offen sc1\n" \
    "LS2_HSW%=:\n" \
    "v_subrev_u32 v65, 768, v52\n" \
    "LS2_HSP%=:\n" \
    "buffer_load_dword v64, v65, s[48:51], 0 offen sc1\n" \
    "s_waitcnt vmcnt(0)\n" \
    "v_readlane_b32 s68, v64, 4\n" \
    "v_cmp_eq_u32 vcc, 0, v64\n" \
    "s_and_b32 s55, vcc_lo, 31\n" \
    "s_cmp_eq_u32 s55, 0\n" \
    "s_cbranch_scc1 LS2_HSD%=\n" \
    "s_add_u32 s67, s67, 1\n" \
    "s_cmp_gt_u32 s67, 65536\n" \
    "s_cbranch_scc1 LS2_HSD%=\n" \
    "s_sleep 2\n" \
    "s_branch LS2_HSP%=\n" \
    "LS2_HSD%=:\n" \
    "s_andn2_b32 s68, s68, 3\n" \
    "v_and_b32 v64, 2, v64\n" \
    "v_lshlrev_b32 v64, 12, v64\n" \
    "v_subrev_u32 v48, 1024, v52\n" \
    "v_lshlrev_b32 v48, 18, v48\n" \
    "v_add_u32 v48, v48, v64\n" \
    "s_mov_b32 s67, 0\n" \
    "s_nop 0\n" \
    "v_readlane_b32 s53, v48, 0\n" \
    "v_readlane_b32 s43, v48, 2\n" \
    "v_mov_b32 v152, -1\n" \
    "v_mov_b32 v153, -1\n" \
    "v_mov_b32 v154, -1\n" \
    "v_mov_b32 v155, -1\n" \
    "v_mov_b64 v[158:159], 0\n" \
    "v_mov_b64 v[162:163], 0\n" \
    "v_mov_b64 v[166:167], 0\n" \
    "v_mov_b64 v[170:171], 0\n" \
    "v_mov_b64 v[174:175], 0\n" \
    "v_mov_b64 v[178:179], 0\n" \
    "v_mov_b32 v176, 0x3f803f80\n" \
    "v_mov_b32 v177, 0x3f803f80\n" \
    "v_mov_b64 v[182:183], 0\n" \
    "LS2_LOOP%=:\n" \
    "s_and_b32 s70, s68, 3\n" \
    "s_andn2_b32 s64, s68, 3\n" \
    "LS2_CWC%=:\n" \
    "s_memrealtime s[72:73]\n" \
    "s_waitcnt lgkmcnt(0)\n" \
    "s_sub_u32 s55, s72, s64\n" \
    "s_cmp_ge_i32 s55, -12\n" \
    "s_cbranch_scc1 LS2_CW%=\n" \
    "s_cmp_lt_i32 s55, -4096\n" \
    "s_cbranch_scc1 LS2_CWO%=\n" \
    "s_sleep 2\n" \
    "s_branch LS2_CWC%=\n" \
    "LS2_CW%=:\n" \
    "s_memrealtime s[72:73]\n" \
    "s_waitcnt lgkmcnt(0)\n" \
    "s_sub_u32 s55, s72, s64\n" \
    "s_cmp_ge_i32 s55, 0\n" \
    "s_cbranch_scc1 LS2_CWO%=\n" \
    "s_memrealtime s[72:73]\n" \
    "s_waitcnt lgkmcnt(0)\n" \
    "s_sub_u32 s55, s72, s64\n" \
    "s_cmp_ge_i32 s55, 0\n" \
    "s_cbranch_scc1 LS2_CWO%=\n" \
    "s_memrealtime s[72:73]\n" \
    "s_waitcnt lgkmcnt(0)\n" \
    "s_sub_u32 s55, s72, s64\n" \
    "s_cmp_ge_i32 s55, 0\n" \
    "s_cbranch_scc1 LS2_CWO%=\n" \
    "s_memrealtime s[72:73]\n" \
    "s_waitcnt lgkmcnt(0)\n" \
    "s_sub_u32 s55, s72, s64\n" \
    "s_cmp_ge_i32 s55, 0\n" \
    "s_cbranch_scc1 LS2_CWO%=\n" \
    "s_branch LS2_CW%=\n" \
    "LS2_CWO%=:\n" \
    "s_mov_b32 s58, s70\n" \
    "LS2_CWD%=:\n" \
    "s_sub_u32 s58, s58, 1\n" \
    "s_cbranch_scc0 LS2_CWD%=\n" \
    "LS2_RETRY%=:\n" \
    "buffer_load_dwordx4 v[64:67], v32, s[44:47], s53 offen sc1\n" \
    "buffer_load_dwordx4 v[68:71], v33, s[44:47], s53 offen sc1\n" \
    "v_mov_b64 v[100:101], 0\n" \
    "v_mov_b64 v[104:105], 0\n" \
    "v_mov_b64 v[102:103], 0\n" \
    "v_mov_b64 v[106:107], 0\n" \
    "s_waitcnt vmcnt(1)\n" \
    "ds_write_b128 v34, v[64:67]\n" \
    "v_smfmac_f32_16x16x64_bf16 v[104:107], v[64:67], a[56:63], v47\n" \
    "v_max3_u32 v136, v64, v65, v66\n" \
    "s_waitcnt vmcnt(0)\n" \
    "ds_write_b128 v35, v[68:71]\n" \
    "v_smfmac_f32_16x16x64_bf16 v[104:107], v[68:71], a[64:71], v47\n" \
    "v_max3_u32 v136, v136, v67, v68\n" \
    "v_max3_u32 v136, v136, v69, v70\n" \
    "v_smfmac_f32_16x16x64_bf16 v[100:103], v[64:67], a[0:7], v47\n" \
    "v_max_u32 v136, v136, v71\n" \
    "v_cmp_eq_u32 vcc, -1, v136\n" \
    "v_smfmac_f32_16x16x64_bf16 v[100:103], v[68:71], a[8:15], v47\n" \
    "s_cbranch_vccnz LS2_SLOW%=\n" \
    "LS2_CONT%=:\n" \
    "s_waitcnt lgkmcnt(0)\n" \
    "s_barrier\n" \
    "ds_read_b128 v[72:75], v36\n" \
    "ds_read_b128 v[76:79], v37\n" \
    "ds_read_b128 v[80:83], v38\n" \
    "ds_read_b128 v[84:87], v39\n" \
    "ds_read_b128 v[88:91], v40\n" \
    "ds_read_b64 v[150:151], v51 offset:7168\n" \
    "s_waitcnt lgkmcnt(5)\n" \
    "v_smfmac_f32_16x16x64_bf16 v[104:107], v[72:75], a[72:79], v47\n" \
    "v_smfmac_f32_16x16x64_bf16 v[100:103], v[72:75], a[16:23], v47\n" \
    "s_waitcnt lgkmcnt(4)\n" \
    "v_smfmac_f32_16x16x64_bf16 v[104:107], v[76:79], a[80:87], v47\n" \
    "v_smfmac_f32_16x16x64_bf16 v[100:103], v[76:79], a[24:31], v47\n" \
    "s_waitcnt lgkmcnt(3)\n" \
    "v_smfmac_f32_16x16x64_bf16 v[104:107], v[80:83], a[88:95], v47\n" \
    "s_waitcnt lgkmcnt(2)\n" \
    "v_smfmac_f32_16x16x64_bf16 v[104:107], v[84:87], a[96:103], v47\n" \
    "s_waitcnt lgkmcnt(1)\n" \
    "v_smfmac_f32_16x16x64_bf16 v[104:107], v[88:91], a[104:111], v47\n" \
    "v_smfmac_f32_16x16x64_bf16 v[100:103], v[80:83], a[32:39], v47\n" \
    "s_nop 2\n" \
    "v_smfmac_f32_16x16x64_bf16 v[100:103], v[84:87], a[40:47], v47\n" \
    "s_nop 2\n" \
    "v_cvt_pk_bf16_f32 v138, v104, v105\n" \
    "v_smfmac_f32_16x16x64_bf16 v[100:103], v[88:91], a[48:55], v47\n" \
    "v_cvt_pk_bf16_f32 v139, v106, v107\n" \
    "ds_write_b64 v44, v[138:139]\n" \
    "s_nop 5\n" \
    "v_cvt_pk_bf16_f32 v140, v100, v101\n" \
    "v_cvt_pk_bf16_f32 v141, v102, v103\n" \
    "ds_write_b64 v43, v[140:141]\n" \
    "s_waitcnt lgkmcnt(0)\n" \
    "s_barrier\n" \
    "s_cmp_eq_u32 s52, 63\n" \
    "s_cbranch_scc1 LS2_END%=\n" \
    "ds_read_b64 v[180:181], v54\n" \
    "s_waitcnt lgkmcnt(0)\n" \
    "v_mfma_f32_16x16x32_bf16 v[132:135], v[180:183], v[176:179], 0\n" \
    "v_mfma_f32_16x16x32_bf16 v[112:115], v[180:183], v[156:159], 0\n" \
    "v_mfma_f32_16x16x32_bf16 v[116:119], v[180:183], v[160:163], 0\n" \
    "v_mfma_f32_16x16x32_bf16 v[120:123], v[180:183], v[164:167], 0\n" \
    "v_mfma_f32_16x16x32_bf16 v[124:127], v[180:183], v[168:171], 0\n" \
    "s_nop 3\n" \
    "v_min_u32 v132, -2, v132\n" \
    "v_min_u32 v133, -2, v133\n" \
    "v_min_u32 v134, -2, v134\n" \
    "v_min_u32 v135, -2, v135\n" \
    "buffer_store_dwordx4 v[132:135], v55, s[44:47], s53 offen sc1\n" \
    "v_cvt_pk_bf16_f32 v112, v112, v113\n" \
    "v_cvt_pk_bf16_f32 v113, v114, v115\n" \
    "v_cvt_pk_bf16_f32 v116, v116, v117\n" \
    "v_cvt_pk_bf16_f32 v117, v118, v119\n" \
    "s_nop 0\n" \
    "v_permlane32_swap_b32 v112, v116\n" \
    "v_permlane32_swap_b32 v113, v117\n" \
    "v_min_u32 v112, -2, v112\n" \
    "v_min_u32 v113, -2, v113\n" \
    "buffer_store_dwordx2 v[112:113], v57, s[44:47], s53 offen sc1\n" \
    "v_cvt_pk_bf16_f32 v120, v120, v121\n" \
    "v_cvt_pk_bf16_f32 v121, v122, v123\n" \
    "v_cvt_pk_bf16_f32 v124, v124, v125\n" \
    "v_cvt_pk_bf16_f32 v125, v126, v127\n" \
    "s_nop 0\n" \
    "v_permlane32_swap_b32 v120, v124\n" \
    "v_permlane32_swap_b32 v121, v125\n" \
    "v_min_u32 v120, -2, v120\n" \
    "v_min_u32 v121, -2, v121\n" \
    "buffer_store_dwordx2 v[120:121], v58, s[44:47], s53 offen sc1\n" \
    "v_readfirstlane_b32 s55, v151\n" \
    "s_max_u32 s41, s41, s55\n" \
    "s_add_u32 s52, s52, 1\n" \
    "s_cmp_lt_u32 s52, 63\n" \
    "s_cselect_b32 s55, 458752, 0\n" \
    "s_add_u32 s59, s59, s55\n" \
    "s_min_u32 s40, s52, 62\n" \
    "s_cmp_lt_u32 s40, s41\n" \
    "s_cbranch_scc1 LS2_F1OK%=\n" \
    "LS2_F1P%=:\n" \
    "buffer_load_dword v138, v52, s[48:51], 0 offen offset:3328 sc1\n" \
    "s_waitcnt vmcnt(0)\n" \
    "v_cmp_gt_u32 vcc, 28, v138\n" \
    "s_ff1_i32_b64 s41, vcc\n" \
    "s_cmp_lt_u32 s40, s41\n" \
    "s_cbranch_scc1 LS2_F1OK%=\n" \
    "s_add_u32 s67, s67, 1\n" \
    "s_cmp_gt_u32 s67, 65536\n" \
    "s_cbranch_scc1 LS2_F1OK%=\n" \
    "s_sleep 4\n" \
    "s_branch LS2_F1P%=\n" \
    "LS2_F1OK%=:\n" \
    "buffer_load_dwordx2 v[156:157], v56, s[48:51], s59 offen offset:0 sc1\n" \
    "buffer_load_dwordx2 v[160:161], v56, s[48:51], s59 offen offset:1024 sc1\n" \
    "buffer_load_dwordx2 v[164:165], v56, s[48:51], s59 offen offset:2048 sc1\n" \
    "buffer_load_dwordx2 v[168:169], v56, s[48:51], s59 offen offset:3072 sc1\n" \
    "s_add_u32 s64, s59, 4096\n" \
    "buffer_load_dwordx2 v[172:173], v56, s[48:51], s64 offen sc1\n" \
    "s_mov_b32 s58, %16\n" \
    "LS2_WS%=:\n" \
    "s_sleep 1\n" \
    "s_sub_u32 s58, s58, 1\n" \
    "s_cbranch_scc0 LS2_WS%=\n" \
    "buffer_store_dwordx2 v[152:153], v57, s[44:47], s43 offen sc1\n" \
    "buffer_store_dwordx2 v[152:153], v58, s[44:47], s43 offen sc1\n" \
    "buffer_store_dwordx4 v[152:155], v55, s[44:47], s43 offen sc1\n" \
    "s_and_b32 s55, s52, 3\n" \
    "s_xor_b32 s64, s55, 2\n" \
    "s_nop 1\n" \
    "v_readlane_b32 s53, v48, s55\n" \
    "v_readlane_b32 s43, v48, s64\n" \
    "v_readfirstlane_b32 s68, v150\n" \
    "s_branch LS2_LOOP%=\n" \
    "LS2_SLOW%=:\n" \
    "s_add_u32 s65, s65, 1\n" \
    "s_cmp_gt_u32 s65, 65536\n" \
    "s_cbranch_scc1 LS2_CONT%=\n" \
    "s_mov_b32 s63, 1\n" \
    "s_nop 7\n" \
    "s_branch LS2_RETRY%=\n" \
    "LS2_END%=:\n" \
    "s_waitcnt vmcnt(0)\n"
#define ASM_SCORE3 \
    "s_mov_b32 s44, %1\n" \
    "s_mov_b32 s45, %2\n" \
    "s_mov_b32 s46, %3\n" \
    "s_mov_b32 s47, %4\n" \
    "s_mov_b32 s48, %5\n" \
    "s_mov_b32 s49, %6\n" \
    "s_mov_b32 s50, %7\n" \
    "s_mov_b32 s51, %8\n" \
    "ds_read_b32 v32, %0 offset:0\n" \
    "ds_read_b32 v33, %0 offset:256\n" \
    "ds_read_b32 v34, %0 offset:512\n" \
    "ds_read_b32 v35, %0 offset:768\n" \
    "ds_read_b32 v36, %0 offset:1024\n" \
    "ds_read_b32 v37, %0 offset:1280\n" \
    "ds_read_b32 v38, %0 offset:1536\n" \
    "ds_read_b32 v39, %0 offset:1792\n" \
    "ds_read_b32 v40, %0 offset:2048\n" \
    "ds_read_b32 v41, %0 offset:2304\n" \
    "ds_read_b32 v42, %0 offset:2560\n" \
    "ds_read_b32 v43, %0 offset:2816\n" \
    "s_waitcnt lgkmcnt(0)\n" \
    "ds_read_b32 v44, %0 offset:3072\n" \
    "ds_read_b32 v45, %0 offset:3328\n" \
    "ds_read_b32 v46, %0 offset:3584\n" \
    "ds_read_b32 v47, %0 offset:3840\n" \
    "ds_read_b32 v48, %0 offset:4096\n" \
    "ds_read_b32 v49, %0 offset:4352\n" \
    "ds_read_b32 v50, %0 offset:4608\n" \
    "ds_read_b32 v51, %0 offset:4864\n" \
    "ds_read_b32 v52, %0 offset:5120\n" \
    "ds_read_b32 v53, %0 offset:5376\n" \
    "ds_read_b32 v54, %0 offset:5632\n" \
    "ds_read_b32 v55, %0 offset:5888\n" \
    "s_waitcnt lgkmcnt(0)\n" \
    "ds_read_b32 v56, %0 offset:6144\n" \
    "ds_read_b32 v57, %0 offset:6400\n" \
    "ds_read_b32 v58, %0 offset:6656\n" \
    "ds_read_b32 v59, %0 offset:6912\n" \
    "ds_read_b32 v60, %0 offset:7168\n" \
    "ds_read_b32 v61, %0 offset:7424\n" \
    "s_mov_b32 s41, 0\n" \
    "s_mov_b32 s40, 0\n" \
    "s_mov_b32 s69, %10\n" \
    "s_mov_b32 s71, %11\n" \
    "s_mov_b32 s56, %12\n" \
    "s_mov_b32 s63, 0\n" \
    "s_mov_b32 s52, 0\n" \
    "s_mov_b32 s65, 0\n" \
    "s_mov_b32 s66, 0\n" \
    "s_mov_b32 s67, 0\n" \
    "s_waitcnt lgkmcnt(0)\n" \
    "s_mov_b32 s59, %15\n" \
    "s_mov_b32 s60, %17\n" \
    "s_mov_b32 s58, %13\n" \
    "s_lshl_b32 s55, s58, 11\n" \
    "s_add_u32 s64, s55, %14\n" \
    "buffer_load_dwordx4 a[0:3], v49, s[48:51], s64 offen\n" \
    "buffer_load_dwordx4 a[4:7], v49, s[48:51], s64 offen offset:1024\n" \
    "s_add_u32 s58, s58, 1\n" \
    "s_cmp_eq_u32 s58, 7\n" \
    "s_cselect_b32 s58, 0, s58\n" \
    "s_lshl_b32 s55, s58, 11\n" \
    "s_add_u32 s64, s55, %14\n" \
    "buffer_load_dwordx4 a[8:11], v49, s[48:51], s64 offen\n" \
    "buffer_load_dwordx4 a[12:15], v49, s[48:51], s64 offen offset:1024\n" \
    "s_add_u32 s58, s58, 1\n" \
    "s_cmp_eq_u32 s58, 7\n" \
    "s_cselect_b32 s58, 0, s58\n" \
    "s_lshl_b32 s55, s58, 11\n" \
    "s_add_u32 s64, s55, %14\n" \
    "buffer_load_dwordx4 a[16:19], v49, s[48:51], s64 offen\n" \
    "buffer_load_dwordx4 a[20:23], v49, s[48:51], s64 offen offset:1024\n" \
    "s_add_u32 s58, s58, 1\n" \
    "s_cmp_eq_u32 s58, 7\n" \
    "s_cselect_b32 s58, 0, s58\n" \
    "s_lshl_b32 s55, s58, 11\n" \
    "s_add_u32 s64, s55, %14\n" \
    "buffer_load_dwordx4 a[24:27], v49, s[48:51], s64 offen\n" \
    "buffer_load_dwordx4 a[28:31], v49, s[48:51], s64 offen offset:1024\n" \
    "s_add_u32 s58, s58, 1\n" \
    "s_cmp_eq_u32 s58, 7\n" \
    "s_cselect_b32 s58, 0, s58\n" \
    "s_lshl_b32 s55, s58, 11\n" \
    "s_add_u32 s64, s55, %14\n" \
    "buffer_load_dwordx4 a[32:35], v49, s[48:51], s64 offen\n" \
    "buffer_load_dwordx4 a[36:39], v49, s[48:51], s64 offen offset:1024\n" \
    "s_add_u32 s58, s58, 1\n" \
    "s_cmp_eq_u32 s58, 7\n" \
    "s_cselect_b32 s58, 0, s58\n" \
    "s_lshl_b32 s55, s58, 11\n" \
    "s_add_u32 s64, s55, %14\n" \
    "buffer_load_dwordx4 a[40:43], v49, s[48:51], s64 offen\n" \
    "buffer_load_dwordx4 a[44:47], v49, s[48:51], s64 offen offset:1024\n" \
    "s_add_u32 s58, s58, 1\n" \
    "s_cmp_eq_u32 s58, 7\n" \
    "s_cselect_b32 s58, 0, s58\n" \
    "s_lshl_b32 s55, s58, 11\n" \
    "s_add_u32 s64, s55, %14\n" \
    "buffer_load_dwordx4 a[48:51], v49, s[48:51], s64 offen\n" \
    "buffer_load_dwordx4 a[52:55], v49, s[48:51], s64 offen offset:1024\n" \
    "s_add_u32 s58, s58, 1\n" \
    "s_cmp_eq_u32 s58, 7\n" \
    "s_cselect_b32 s58, 0, s58\n" \
    "s_cmp_lt_u32 s40, s41\n" \
    "s_cbranch_scc1 LS3_F0OK%=\n" \
    "LS3_F0P%=:\n" \
    "buffer_load_dword v138, v52, s[48:51], 0 offen offset:3072 sc1\n" \
    "s_waitcnt vmcnt(0)\n" \
    "v_cmp_gt_u32 vcc, 28, v138\n" \
    "s_ff1_i32_b64 s41, vcc\n" \
    "s_cmp_lt_u32 s40, s41\n" \
    "s_cbranch_scc1 LS3_F0OK%=\n" \
    "s_add_u32 s67, s67, 1\n" \
    "s_cmp_gt_u32 s67, 65536\n" \
    "s_cbranch_scc1 LS3_F0OK%=\n" \
    "s_sleep 4\n" \
    "s_branch LS3_F0P%=\n" \
    "LS3_F0OK%=:\n" \
    "s_add_u32 s64, s59, 12288\n" \
    "buffer_load_dwordx4 a[56:59], v49, s[48:51], s64 offen sc1\n" \
    "buffer_load_dwordx4 a[60:63], v49, s[48:51], s64 offen offset:1024 sc1\n" \
    "s_add_u32 s64, s59, 0\n" \
    "buffer_load_dwordx4 a[64:67], v49, s[48:51], s64 offen sc1\n" \
    "buffer_load_dwordx4 a[68:71], v49, s[48:51], s64 offen offset:1024 sc1\n" \
    "s_add_u32 s64, s59, 2048\n" \
    "buffer_load_dwordx4 a[72:75], v49, s[48:51], s64 offen sc1\n" \
    "buffer_load_dwordx4 a[76:79], v49, s[48:51], s64 offen offset:1024 sc1\n" \
    "s_add_u32 s64, s59, 4096\n" \
    "buffer_load_dwordx4 a[80:83], v49, s[48:51], s64 offen sc1\n" \
    "buffer_load_dwordx4 a[84:87], v49, s[48:51], s64 offen offset:1024 sc1\n" \
    "s_add_u32 s64, s59, 6144\n" \
    "buffer_load_dwordx4 a[88:91], v49, s[48:51], s64 offen sc1\n" \
    "buffer_load_dwordx4 a[92:95], v49, s[48:51], s64 offen offset:1024 sc1\n" \
    "s_add_u32 s64, s59, 8192\n" \
    "buffer_load_dwordx4 a[96:99], v49, s[48:51], s64 offen sc1\n" \
    "buffer_load_dwordx4 a[100:103], v49, s[48:51], s64 offen offset:1024 sc1\n" \
    "s_add_u32 s64, s59, 10240\n" \
    "buffer_load_dwordx4 a[104:107], v49, s[48:51], s64 offen sc1\n" \
    "buffer_load_dwordx4 a[108:111], v49, s[48:51], s64 offen offset:1024 sc1\n" \
    "s_waitcnt vmcnt(0)\n" \
    "s_barrier\n" \
    "v_mov_b32 v64, 1\n" \
    "buffer_atomic_add v64, v53, s[48:51], 0 offen sc0\n" \
    "s_waitcnt vmcnt(0)\n" \
    "v_readfirstlane_b32 s55, v64\n" \
    "s_cmp_lg_u32 s55, 119\n" \
    "s_cbranch_scc1 LS3_HSW%=\n" \
    "s_memrealtime s[72:73]\n" \
    "s_waitcnt lgkmcnt(0)\n" \
    "s_add_u32 s55, s72, %9\n" \
    "s_andn2_b32 s55, s55, 3\n" \
    "s_add_u32 s64, s55, s56\n" \
    "v_mov_b32 v68, s64\n" \
    "v_mov_b32 v69, 0\n" \
    "v_subrev_u32 v65, 1024, v52\n" \
    "v_lshrrev_b32 v66, 3, v65\n" \
    "v_mul_u32_u24 v66, 163840, v66\n" \
    "v_and_b32 v67, 4, v65\n" \
    "v_lshlrev_b32 v67, 11, v67\n" \
    "v_add3_u32 v66, v66, v67, v67\n" \
    "v_sub_u32 v66, v66, v67\n" \
    "v_add_u32 v66, 7168, v66\n" \
    "v_mov_b32 v67, 0x7fffff00\n" \
    "v_cmp_gt_u32 vcc, 32, v65\n" \
    "s_nop 1\n" \
    "v_cndmask_b32 v66, v67, v66, vcc\n" \
    "buffer_store_dwordx2 v[68:69], v66, s[44:47], 0 offen sc1\n" \
    "s_or_b32 s55, s55, 1\n" \
    "v_mov_b32 v65, s55\n" \
    "s_waitcnt vmcnt(0)\n" \
    "v_subrev_u32 v66, 240, v53\n" \
    "buffer_store_dword v65, v66, s[48:51], 0 offen sc1\n" \
    "LS3_HSW%=:\n" \
    "v_subrev_u32 v65, 768, v52\n" \
    "LS3_HSP%=:\n" \
    "buffer_load_dword v64, v65, s[48:51], 0 offen sc1\n" \
    "s_waitcnt vmcnt(0)\n" \
    "v_readlane_b32 s68, v64, 4\n" \
    "v_cmp_eq_u32 vcc, 0, v64\n" \
    "s_and_b32 s55, vcc_lo, 31\n" \
    "s_cmp_eq_u32 s55, 0\n" \
    "s_cbranch_scc1 LS3_HSD%=\n" \
    "s_add_u32 s67, s67, 1\n" \
    "s_cmp_gt_u32 s67, 65536\n" \
    "s_cbranch_scc1 LS3_HSD%=\n" \
    "s_sleep 2\n" \
    "s_branch LS3_HSP%=\n" \
    "LS3_HSD%=:\n" \
    "s_andn2_b32 s68, s68, 3\n" \
    "v_and_b32 v64, 2, v64\n" \
    "v_lshlrev_b32 v64, 12, v64\n" \
    "v_subrev_u32 v48, 1024, v52\n" \
    "v_lshlrev_b32 v48, 18, v48\n" \
    "v_add_u32 v48, v48, v64\n" \
    "s_mov_b32 s67, 0\n" \
    "s_nop 0\n" \
    "v_readlane_b32 s53, v48, 0\n" \
    "v_readlane_b32 s43, v48, 2\n" \
    "v_mov_b32 v152, -1\n" \
    "v_mov_b32 v153, -1\n" \
    "v_mov_b32 v154, -1\n" \
    "v_mov_b32 v155, -1\n" \
    "LS3_LOOP%=:\n" \
    "s_and_b32 s70, s68, 3\n" \
    "s_andn2_b32 s64, s68, 3\n" \
    "LS3_CWC%=:\n" \
    "s_memrealtime s[72:73]\n" \
    "s_waitcnt lgkmcnt(0)\n" \
    "s_sub_u32 s55, s72, s64\n" \
    "s_cmp_ge_i32 s55, -12\n" \
    "s_cbranch_scc1 LS3_CW%=\n" \
    "s_cmp_lt_i32 s55, -4096\n" \
    "s_cbranch_scc1 LS3_CWO%=\n" \
    "s_sleep 2\n" \
    "s_branch LS3_CWC%=\n" \
    "LS3_CW%=:\n" \
    "s_memrealtime s[72:73]\n" \
    "s_waitcnt lgkmcnt(0)\n" \
    "s_sub_u32 s55, s72, s64\n" \
    "s_cmp_ge_i32 s55, 0\n" \
    "s_cbranch_scc1 LS3_CWO%=\n" \
    "s_memrealtime s[72:73]\n" \
    "s_waitcnt lgkmcnt(0)\n" \
    "s_sub_u32 s55, s72, s64\n" \
    "s_cmp_ge_i32 s55, 0\n" \
    "s_cbranch_scc1 LS3_CWO%=\n" \
    "s_memrealtime s[72:73]\n" \
    "s_waitcnt lgkmcnt(0)\n" \
    "s_sub_u32 s55, s72, s64\n" \
    "s_cmp_ge_i32 s55, 0\n" \
    "s_cbranch_scc1 LS3_CWO%=\n" \
    "s_memrealtime s[72:73]\n" \
    "s_waitcnt lgkmcnt(0)\n" \
    "s_sub_u32 s55, s72, s64\n" \
    "s_cmp_ge_i32 s55, 0\n" \
    "s_cbranch_scc1 LS3_CWO%=\n" \
    "s_branch LS3_CW%=\n" \
    "LS3_CWO%=:\n" \
    "s_mov_b32 s58, s70\n" \
    "LS3_CWD%=:\n" \
    "s_sub_u32 s58, s58, 1\n" \
    "s_cbranch_scc0 LS3_CWD%=\n" \
    "LS3_RETRY%=:\n" \
    "buffer_load_dwordx4 v[64:67], v32, s[44:47], s53 offen sc1\n" \
    "buffer_load_dwordx2 v[150:151], v33, s[44:47], s53 offen sc1\n" \
    "v_mov_b64 v[100:101], 0\n" \
    "v_mov_b64 v[104:105], 0\n" \
    "v_mov_b64 v[108:109], 0\n" \
    "v_mov_b64 v[102:103], 0\n" \
    "v_mov_b64 v[106:107], 0\n" \
    "v_mov_b64 v[110:111], 0\n" \
    "s_waitcnt vmcnt(1)\n" \
    "ds_write_b128 v34, v[64:67]\n" \
    "v_smfmac_f32_16x16x64_bf16 v[104:107], v[64:67], a[56:63], v47\n" \
    "v_max3_u32 v136, v64, v65, v66\n" \
    "s_waitcnt vmcnt(0)\n" \
    "ds_write_b64 v34, v[150:151] offset:1024\n" \
    "v_smfmac_f32_16x16x64_bf16 v[100:103], v[64:67], a[0:7], v47\n" \
    "v_max3_u32 v136, v136, v67, v150\n" \
    "v_max_u32 v136, v136, v151\n" \
    "v_cmp_eq_u32 vcc, -1, v136\n" \
    "s_cbranch_vccnz LS3_SLOW%=\n" \
    "LS3_CONT%=:\n" \
    "s_waitcnt lgkmcnt(0)\n" \
    "s_barrier\n" \
    "ds_read_b128 v[68:71], v35\n" \
    "ds_read_b128 v[72:75], v36\n" \
    "ds_read_b128 v[76:79], v37\n" \
    "ds_read_b128 v[80:83], v38\n" \
    "ds_read_b128 v[84:87], v39\n" \
    "ds_read_b128 v[88:91], v40\n" \
    "ds_read_b128 v[92:95], v41\n" \
    "ds_read_b128 v[96:99], v42\n" \
    "s_waitcnt lgkmcnt(7)\n" \
    "v_smfmac_f32_16x16x64_bf16 v[104:107], v[68:71], a[64:71], v47\n" \
    "s_waitcnt lgkmcnt(6)\n" \
    "v_smfmac_f32_16x16x64_bf16 v[104:107], v[72:75], a[72:79], v47\n" \
    "s_waitcnt lgkmcnt(5)\n" \
    "v_smfmac_f32_16x16x64_bf16 v[104:107], v[76:79], a[80:87], v47\n" \
    "s_waitcnt lgkmcnt(4)\n" \
    "v_smfmac_f32_16x16x64_bf16 v[104:107], v[80:83], a[88:95], v47\n" \
    "s_waitcnt lgkmcnt(3)\n" \
    "v_smfmac_f32_16x16x64_bf16 v[104:107], v[84:87], a[96:103], v47\n" \
    "s_waitcnt lgkmcnt(2)\n" \
    "v_smfmac_f32_16x16x64_bf16 v[104:107], v[88:91], a[104:111], v47\n" \
    "v_smfmac_f32_16x16x64_bf16 v[100:103], v[68:71], a[8:15], v47\n" \
    "s_nop 2\n" \
    "v_smfmac_f32_16x16x64_bf16 v[100:103], v[72:75], a[16:23], v47\n" \
    "s_nop 2\n" \
    "v_smfmac_f32_16x16x64_bf16 v[100:103], v[76:79], a[24:31], v47\n" \
    "v_permlane32_swap_b32 v104, v105\n" \
    "v_permlane32_swap_b32 v106, v107\n" \
    "v_smfmac_f32_16x16x64_bf16 v[100:103], v[80:83], a[32:39], v47\n" \
    "v_add_f32 v138, v104, v105\n" \
    "v_add_f32 v139, v106, v107\n" \
    "s_nop 0\n" \
    "v_smfmac_f32_16x16x64_bf16 v[100:103], v[84:87], a[40:47], v47\n" \
    "v_max_f32_dpp v140, v138, v138 quad_perm:[1,0,3,2] row_mask:0xf bank_mask:0xf\n" \
    "v_max_f32_dpp v141, v139, v139 quad_perm:[1,0,3,2] row_mask:0xf bank_mask:0xf\n" \
    "s_nop 0\n" \
    "v_smfmac_f32_16x16x64_bf16 v[100:103], v[88:91], a[48:55], v47\n" \
    "v_max_f32_dpp v140, v140, v140 quad_perm:[2,3,0,1] row_mask:0xf bank_mask:0xf\n" \
    "v_max_f32_dpp v141, v141, v141 quad_perm:[2,3,0,1] row_mask:0xf bank_mask:0xf\n" \
    "s_nop 0\n" \
    "v_max_f32_dpp v140, v140, v140 row_ror:4 row_mask:0xf bank_mask:0xf\n" \
    "v_max_f32_dpp v141, v141, v141 row_ror:4 row_mask:0xf bank_mask:0xf\n" \
    "s_nop 0\n" \
    "v_max_f32_dpp v140, v140, v140 row_ror:8 row_mask:0xf bank_mask:0xf\n" \
    "v_max_f32_dpp v141, v141, v141 row_ror:8 row_mask:0xf bank_mask:0xf\n" \
    "v_cvt_pk_bf16_f32 v144, v100, v101\n" \
    "v_cvt_pk_bf16_f32 v145, v102, v103\n" \
    "ds_write_b64 v43, v[144:145]\n" \
    "v_pk_add_f32 v[138:139], v[138:139], v[140:141] neg_lo:[0,1] neg_hi:[0,1]\n" \
    "v_exp_f32 v138, v138\n" \
    "v_exp_f32 v139, v139\n" \
    "v_min_u32 v140, -2, v140\n" \
    "v_cvt_pk_bf16_f32 v142, v138, v139\n" \
    "ds_write_b16 v54, v142\n" \
    "ds_write_b16_d16_hi v54, v142 offset:64\n" \
    "v_min_u32 v141, -2, v141\n" \
    "s_waitcnt lgkmcnt(0)\n" \
    "s_barrier\n" \
    "s_cmp_eq_u32 s52, 63\n" \
    "ds_read_b64_tr_b16 v[112:113], v45 offset:0\n" \
    "ds_read_b64_tr_b16 v[114:115], v50 offset:0\n" \
    "ds_read_b64_tr_b16 v[116:117], v45 offset:1024\n" \
    "ds_read_b64_tr_b16 v[118:119], v50 offset:1024\n" \
    "ds_read_b64_tr_b16 v[120:121], v45 offset:2048\n" \
    "ds_read_b64_tr_b16 v[122:123], v50 offset:2048\n" \
    "ds_read_b64_tr_b16 v[124:125], v45 offset:3072\n" \
    "ds_read_b64_tr_b16 v[126:127], v50 offset:3072\n" \
    "s_cbranch_scc1 LS3_LAST%=\n" \
    "buffer_store_dwordx2 v[140:141], v55, s[44:47], s53 offen sc1\n" \
    "s_waitcnt lgkmcnt(4)\n" \
    "v_smfmac_f32_16x16x64_bf16 v[108:111], v[92:95], v[112:119], v47\n" \
    "s_waitcnt lgkmcnt(0)\n" \
    "v_smfmac_f32_16x16x64_bf16 v[108:111], v[96:99], v[120:127], v47\n" \
    "s_nop 7\n" \
    "v_permlane32_swap_b32 v108, v110\n" \
    "v_permlane32_swap_b32 v109, v111\n" \
    "v_add_f32 v138, v108, v110\n" \
    "v_add_f32 v139, v109, v111\n" \
    "s_nop 0\n" \
    "v_add_f32_dpp v138, v138, v138 row_ror:8 row_mask:0xf bank_mask:0xf\n" \
    "v_add_f32_dpp v139, v139, v139 row_ror:8 row_mask:0xf bank_mask:0xf\n" \
    "s_mov_b32 vcc_lo, 0x55555555\n" \
    "s_mov_b32 vcc_hi, 0x55555555\n" \
    "v_cndmask_b32_dpp v142, v139, v138, vcc quad_perm:[1,0,3,2] row_mask:0xf bank_mask:0xf\n" \
    "s_not_b64 vcc, vcc\n" \
    "v_cndmask_b32_dpp v143, v138, v139, vcc quad_perm:[1,0,3,2] row_mask:0xf bank_mask:0xf\n" \
    "v_min_u32 v142, -2, v142\n" \
    "v_min_u32 v143, -2, v143\n" \
    "buffer_store_dwordx2 v[142:143], v46, s[44:47], s53 offen sc1\n" \
    "v_readfirstlane_b32 s55, v151\n" \
    "s_max_u32 s41, s41, s55\n" \
    "s_add_u32 s52, s52, 1\n" \
    "s_cmp_lt_u32 s52, 63\n" \
    "s_cselect_b32 s55, 458752, 0\n" \
    "s_add_u32 s59, s59, s55\n" \
    "s_min_u32 s40, s52, 62\n" \
    "s_cmp_lt_u32 s40, s41\n" \
    "s_cbranch_scc1 LS3_F1OK%=\n" \
    "LS3_F1P%=:\n" \
    "buffer_load_dword v138, v52, s[48:51], 0 offen offset:3072 sc1\n" \
    "s_waitcnt vmcnt(0)\n" \
    "v_cmp_gt_u32 vcc, 28, v138\n" \
    "s_ff1_i32_b64 s41, vcc\n" \
    "s_cmp_lt_u32 s40, s41\n" \
    "s_cbranch_scc1 LS3_F1OK%=\n" \
    "s_add_u32 s67, s67, 1\n" \
    "s_cmp_gt_u32 s67, 65536\n" \
    "s_cbranch_scc1 LS3_F1OK%=\n" \
    "s_sleep 4\n" \
    "s_branch LS3_F1P%=\n" \
    "LS3_F1OK%=:\n" \
    "s_add_u32 s64, s59, 12288\n" \
    "buffer_load_dwordx4 a[56:59], v49, s[48:51], s64 offen sc1\n" \
    "buffer_load_dwordx4 a[60:63], v49, s[48:51], s64 offen offset:1024 sc1\n" \
    "s_add_u32 s64, s59, 0\n" \
    "buffer_load_dwordx4 a[64:67], v49, s[48:51], s64 offen sc1\n" \
    "buffer_load_dwordx4 a[68:71], v49, s[48:51], s64 offen offset:1024 sc1\n" \
    "s_add_u32 s64, s59, 2048\n" \
    "buffer_load_dwordx4 a[72:75], v49, s[48:51], s64 offen sc1\n" \
    "buffer_load_dwordx4 a[76:79], v49, s[48:51], s64 offen offset:1024 sc1\n" \
    "s_add_u32 s64, s59, 4096\n" \
    "buffer_load_dwordx4 a[80:83], v49, s[48:51], s64 offen sc1\n" \
    "buffer_load_dwordx4 a[84:87], v49, s[48:51], s64 offen offset:1024 sc1\n" \
    "s_add_u32 s64, s59, 6144\n" \
    "buffer_load_dwordx4 a[88:91], v49, s[48:51], s64 offen sc1\n" \
    "buffer_load_dwordx4 a[92:95], v49, s[48:51], s64 offen offset:1024 sc1\n" \
    "s_add_u32 s64, s59, 8192\n" \
    "buffer_load_dwordx4 a[96:99], v49, s[48:51], s64 offen sc1\n" \
    "buffer_load_dwordx4 a[100:103], v49, s[48:51], s64 offen offset:1024 sc1\n" \
    "s_add_u32 s64, s59, 10240\n" \
    "buffer_load_dwordx4 a[104:107], v49, s[48:51], s64 offen sc1\n" \
    "buffer_load_dwordx4 a[108:111], v49, s[48:51], s64 offen offset:1024 sc1\n" \
    "s_mov_b32 s58, %16\n" \
    "LS3_WS%=:\n" \
    "s_sleep 1\n" \
    "s_sub_u32 s58, s58, 1\n" \
    "s_cbranch_scc0 LS3_WS%=\n" \
    "buffer_store_dwordx2 v[152:153], v46, s[44:47], s43 offen sc1\n" \
    "buffer_store_dwordx2 v[152:153], v55, s[44:47], s43 offen sc1\n" \
    "buffer_store_dwordx4 v[112:115], v49, s[48:51], s60 offen offset:0 sc1\n" \
    "buffer_store_dwordx4 v[116:119], v49, s[48:51], s60 offen offset:1024 sc1\n" \
    "buffer_store_dwordx4 v[120:123], v49, s[48:51], s60 offen offset:2048 sc1\n" \
    "buffer_store_dwordx4 v[124:127], v49, s[48:51], s60 offen offset:3072 sc1\n" \
    "s_add_u32 s60, s60, 262144\n" \
    "s_and_b32 s55, s52, 3\n" \
    "s_xor_b32 s64, s55, 2\n" \
    "s_nop 1\n" \
    "v_readlane_b32 s53, v48, s55\n" \
    "v_readlane_b32 s43, v48, s64\n" \
    "v_readfirstlane_b32 s68, v150\n" \
    "s_branch LS3_LOOP%=\n" \
    "LS3_SLOW%=:\n" \
    "s_add_u32 s65, s65, 1\n" \
    "s_cmp_gt_u32 s65, 65536\n" \
    "s_cbranch_scc1 LS3_CONT%=\n" \
    "s_mov_b32 s63, 1\n" \
    "s_nop 7\n" \
    "s_branch LS3_RETRY%=\n" \
    "LS3_LAST%=:\n" \
    "s_waitcnt lgkmcnt(0)\n" \
    "buffer_store_dwordx4 v[112:115], v49, s[48:51], s60 offen offset:0 sc1\n" \
    "buffer_store_dwordx4 v[116:119], v49, s[48:51], s60 offen offset:1024 sc1\n" \
    "buffer_store_dwordx4 v[120:123], v49, s[48:51], s60 offen offset:2048 sc1\n" \
    "buffer_store_dwordx4 v[124:127], v49, s[48:51], s60 offen offset:3072 sc1\n" \
    "LS3_END%=:\n" \
    "s_waitcnt vmcnt(0)\n"
#define ASM_OUT0 \
    "s_mov_b32 s44, %1\n" \
    "s_mov_b32 s45, %2\n" \
    "s_mov_b32 s46, %3\n" \
    "s_mov_b32 s47, %4\n" \
    "s_mov_b32 s48, %5\n" \
    "s_mov_b32 s49, %6\n" \
    "s_mov_b32 s50, %7\n" \
    "s_mov_b32 s51, %8\n" \
    "ds_read_b32 v32, %0 offset:0\n" \
    "ds_read_b32 v33, %0 offset:256\n" \
    "ds_read_b32 v34, %0 offset:512\n" \
    "ds_read_b32 v35, %0 offset:768\n" \
    "ds_read_b32 v36, %0 offset:1024\n" \
    "ds_read_b32 v37, %0 offset:1280\n" \
    "ds_read_b32 v38, %0 offset:1536\n" \
    "ds_read_b32 v39, %0 offset:1792\n" \
    "ds_read_b32 v40, %0 offset:2048\n" \
    "ds_read_b32 v41, %0 offset:2304\n" \
    "ds_read_b32 v42, %0 offset:2560\n" \
    "ds_read_b32 v43, %0 offset:2816\n" \
    "s_waitcnt lgkmcnt(0)\n" \
    "ds_read_b32 v44, %0 offset:3072\n" \
    "ds_read_b32 v45, %0 offset:3328\n" \
    "ds_read_b32 v46, %0 offset:3584\n" \
    "ds_read_b32 v47, %0 offset:3840\n" \
    "ds_read_b32 v48, %0 offset:4096\n" \
    "ds_read_b32 v49, %0 offset:4352\n" \
    "ds_read_b32 v50, %0 offset:4608\n" \
    "ds_read_b32 v51, %0 offset:4864\n" \
    "ds_read_b32 v52, %0 offset:5120\n" \
    "ds_read_b32 v53, %0 offset:5376\n" \
    "ds_read_b32 v54, %0 offset:5632\n" \
    "ds_read_b32 v55, %0 offset:5888\n" \
    "s_waitcnt lgkmcnt(0)\n" \
    "ds_read_b32 v56, %0 offset:6144\n" \
    "ds_read_b32 v57, %0 offset:6400\n" \
    "ds_read_b32 v58, %0 offset:6656\n" \
    "ds_read_b32 v59, %0 offset:6912\n" \
    "ds_read_b32 v60, %0 offset:7168\n" \
    "ds_read_b32 v61, %0 offset:7424\n" \
    "s_mov_b32 s41, 0\n" \
    "s_mov_b32 s40, 0\n" \
    "s_mov_b32 s69, %10\n" \
    "s_mov_b32 s71, %11\n" \
    "s_mov_b32 s56, %12\n" \
    "s_mov_b32 s63, 0\n" \
    "s_mov_b32 s52, 0\n" \
    "s_mov_b32 s65, 0\n" \
    "s_mov_b32 s66, 0\n" \
    "s_mov_b32 s67, 0\n" \
    "s_waitcnt lgkmcnt(0)\n" \
    "s_mov_b32 s60, %16\n" \
    "s_mov_b32 s62, %15\n" \
    "s_mov_b32 s42, %19\n" \
    "s_mov_b32 s61, 0\n" \
    "s_mov_b32 s58, %13\n" \
    "s_lshl_b32 s55, s58, 11\n" \
    "s_add_u32 s64, s55, %14\n" \
    "buffer_load_dwordx4 a[0:3], v54, s[48:51], s64 offen\n" \
    "buffer_load_dwordx4 a[4:7], v54, s[48:51], s64 offen offset:1024\n" \
    "s_add_u32 s64, s64, 401408\n" \
    "buffer_load_dwordx4 a[56:59], v54, s[48:51], s64 offen\n" \
    "buffer_load_dwordx4 a[60:63], v54, s[48:51], s64 offen offset:1024\n" \
    "s_add_u32 s64, s64, 401408\n" \
    "buffer_load_dwordx4 a[112:115], v54, s[48:51], s64 offen\n" \
    "buffer_load_dwordx4 a[116:119], v54, s[48:51], s64 offen offset:1024\n" \
    "s_add_u32 s64, s64, 401408\n" \
    "buffer_load_dwordx4 a[168:171], v54, s[48:51], s64 offen\n" \
    "buffer_load_dwordx4 a[172:175], v54, s[48:51], s64 offen offset:1024\n" \
    "s_add_u32 s58, s58, 1\n" \
    "s_cmp_eq_u32 s58, 7\n" \
    "s_cselect_b32 s58, 0, s58\n" \
    "s_lshl_b32 s55, s58, 11\n" \
    "s_add_u32 s64, s55, %14\n" \
    "buffer_load_dwordx4 a[8:11], v54, s[48:51], s64 offen\n" \
    "buffer_load_dwordx4 a[12:15], v54, s[48:51], s64 offen offset:1024\n" \
    "s_add_u32 s64, s64, 401408\n" \
    "buffer_load_dwordx4 a[64:67], v54, s[48:51], s64 offen\n" \
    "buffer_load_dwordx4 a[68:71], v54, s[48:51], s64 offen offset:1024\n" \
    "s_add_u32 s64, s64, 401408\n" \
    "buffer_load_dwordx4 a[120:123], v54, s[48:51], s64 offen\n" \
    "buffer_load_dwordx4 a[124:127], v54, s[48:51], s64 offen offset:1024\n" \
    "s_add_u32 s64, s64, 401408\n" \
    "buffer_load_dwordx4 a[176:179], v54, s[48:51], s64 offen\n" \
    "buffer_load_dwordx4 a[180:183], v54, s[48:51], s64 offen offset:1024\n" \
    "s_add_u32 s58, s58, 1\n" \
    "s_cmp_eq_u32 s58, 7\n" \
    "s_cselect_b32 s58, 0, s58\n" \
    "s_lshl_b32 s55, s58, 11\n" \
    "s_add_u32 s64, s55, %14\n" \
    "buffer_load_dwordx4 a[16:19], v54, s[48:51], s64 offen\n" \
    "buffer_load_dwordx4 a[20:23], v54, s[48:51], s64 offen offset:1024\n" \
    "s_add_u32 s64, s64, 401408\n" \
    "buffer_load_dwordx4 a[72:75], v54, s[48:51], s64 offen\n" \
    "buffer_load_dwordx4 a[76:79], v54, s[48:51], s64 offen offset:1024\n" \
    "s_add_u32 s64, s64, 401408\n" \
    "buffer_load_dwordx4 a[128:131], v54, s[48:51], s64 offen\n" \
    "buffer_load_dwordx4 a[132:135], v54, s[48:51], s64 offen offset:1024\n" \
    "s_add_u32 s64, s64, 401408\n" \
    "buffer_load_dwordx4 a[184:187], v54, s[48:51], s64 offen\n" \
    "buffer_load_dwordx4 a[188:191], v54, s[48:51], s64 offen offset:1024\n" \
    "s_add_u32 s58, s58, 1\n" \
    "s_cmp_eq_u32 s58, 7\n" \
    "s_cselect_b32 s58, 0, s58\n" \
    "s_lshl_b32 s55, s58, 11\n" \
    "s_add_u32 s64, s55, %14\n" \
    "buffer_load_dwordx4 a[24:27], v54, s[48:51], s64 offen\n" \
    "buffer_load_dwordx4 a[28:31], v54, s[48:51], s64 offen offset:1024\n" \
    "s_add_u32 s64, s64, 401408\n" \
    "buffer_load_dwordx4 a[80:83], v54, s[48:51], s64 offen\n" \
    "buffer_load_dwordx4 a[84:87], v54, s[48:51], s64 offen offset:1024\n" \
    "s_add_u32 s64, s64, 401408\n" \
    "buffer_load_dwordx4 a[136:139], v54, s[48:51], s64 offen\n" \
    "buffer_load_dwordx4 a[140:143], v54, s[48:51], s64 offen offset:1024\n" \
    "s_add_u32 s64, s64, 401408\n" \
    "buffer_load_dwordx4 a[192:195], v54, s[48:51], s64 offen\n" \
    "buffer_load_dwordx4 a[196:199], v54, s[48:51], s64 offen offset:1024\n" \
    "s_add_u32 s58, s58, 1\n" \
    "s_cmp_eq_u32 s58, 7\n" \
    "s_cselect_b32 s58, 0, s58\n" \
    "s_waitcnt vmcnt(8)\n" \
    "s_lshl_b32 s55, s58, 11\n" \
    "s_add_u32 s64, s55, %14\n" \
    "buffer_load_dwordx4 a[32:35], v54, s[48:51], s64 offen\n" \
    "buffer_load_dwordx4 a[36:39], v54, s[48:51], s64 offen offset:1024\n" \
    "s_add_u32 s64, s64, 401408\n" \
    "buffer_load_dwordx4 a[88:91], v54, s[48:51], s64 offen\n" \
    "buffer_load_dwordx4 a[92:95], v54, s[48:51], s64 offen offset:1024\n" \
    "s_add_u32 s64, s64, 401408\n" \
    "buffer_load_dwordx4 a[144:147], v54, s[48:51], s64 offen\n" \
    "buffer_load_dwordx4 a[148:151], v54, s[48:51], s64 offen offset:1024\n" \
    "s_add_u32 s64, s64, 401408\n" \
    "buffer_load_dwordx4 a[200:203], v54, s[48:51], s64 offen\n" \
    "buffer_load_dwordx4 a[204:207], v54, s[48:51], s64 offen offset:1024\n" \
    "s_add_u32 s58, s58, 1\n" \
    "s_cmp_eq_u32 s58, 7\n" \
    "s_cselect_b32 s58, 0, s58\n" \
    "s_lshl_b32 s55, s58, 11\n" \
    "s_add_u32 s64, s55, %14\n" \
    "buffer_load_dwordx4 a[40:43], v54, s[48:51], s64 offen\n" \
    "buffer_load_dwordx4 a[44:47], v54, s[48:51], s64 offen offset:1024\n" \
    "s_add_u32 s64, s64, 401408\n" \
    "buffer_load_dwordx4 a[96:99], v54, s[48:51], s64 offen\n" \
    "buffer_load_dwordx4 a[100:103], v54, s[48:51], s64 offen offset:1024\n" \
    "s_add_u32 s64, s64, 401408\n" \
    "buffer_load_dwordx4 a[152:155], v54, s[48:51], s64 offen\n" \
    "buffer_load_dwordx4 a[156:159], v54, s[48:51], s64 offen offset:1024\n" \
    "s_add_u32 s64, s64, 401408\n" \
    "buffer_load_dwordx4 a[208:211], v54, s[48:51], s64 offen\n" \
    "buffer_load_dwordx4 a[212:215], v54, s[48:51], s64 offen offset:1024\n" \
    "s_add_u32 s58, s58, 1\n" \
    "s_cmp_eq_u32 s58, 7\n" \
    "s_cselect_b32 s58, 0, s58\n" \
    "s_lshl_b32 s55, s58, 11\n" \
    "s_add_u32 s64, s55, %14\n" \
    "buffer_load_dwordx4 a[48:51], v54, s[48:51], s64 offen\n" \
    "buffer_load_dwordx4 a[52:55], v54, s[48:51], s64 offen offset:1024\n" \
    "s_add_u32 s64, s64, 401408\n" \
    "buffer_load_dwordx4 a[104:107], v54, s[48:51], s64 offen\n" \
    "buffer_load_dwordx4 a[108:111], v54, s[48:51], s64 offen offset:1024\n" \
    "s_add_u32 s64, s64, 401408\n" \
    "buffer_load_dwordx4 a[160:163], v54, s[48:51], s64 offen\n" \
    "buffer_load_dwordx4 a[164:167], v54, s[48:51], s64 offen offset:1024\n" \
    "s_add_u32 s64, s64, 401408\n" \
    "buffer_load_dwordx4 a[216:219], v54, s[48:51], s64 offen\n" \
    "buffer_load_dwordx4 a[220:223], v54, s[48:51], s64 offen offset:1024\n" \
    "s_add_u32 s58, s58, 1\n" \
    "s_cmp_eq_u32 s58, 7\n" \
    "s_cselect_b32 s58, 0, s58\n" \
    "s_waitcnt vmcnt(0)\n" \
    "s_barrier\n" \
    "v_mov_b32 v64, 1\n" \
    "buffer_atomic_add v64, v60, s[48:51], 0 offen sc0\n" \
    "s_waitcnt vmcnt(0)\n" \
    "v_readfirstlane_b32 s55, v64\n" \
    "s_cmp_lg_u32 s55, 119\n" \
    "s_cbranch_scc1 LO0_HSW%=\n" \
    "s_memrealtime s[72:73]\n" \
    "s_waitcnt lgkmcnt(0)\n" \
    "s_add_u32 s55, s72, %9\n" \
    "s_andn2_b32 s55, s55, 3\n" \
    "s_add_u32 s64, s55, s56\n" \
    "v_mov_b32 v68, s64\n" \
    "v_mov_b32 v69, 0\n" \
    "v_subrev_u32 v65, 1024, v59\n" \
    "v_lshrrev_b32 v66, 3, v65\n" \
    "v_mul_u32_u24 v66, 163840, v66\n" \
    "v_and_b32 v67, 4, v65\n" \
    "v_lshlrev_b32 v67, 11, v67\n" \
    "v_add3_u32 v66, v66, v67, v67\n" \
    "v_sub_u32 v66, v66, v67\n" \
    "v_add_u32 v66, 7168, v66\n" \
    "v_mov_b32 v67, 0x7fffff00\n" \
    "v_cmp_gt_u32 vcc, 32, v65\n" \
    "s_nop 1\n" \
    "v_cndmask_b32 v66, v67, v66, vcc\n" \
    "buffer_store_dwordx2 v[68:69], v66, s[44:47], 0 offen sc1\n" \
    "s_or_b32 s55, s55, 1\n" \
    "v_mov_b32 v65, s55\n" \
    "s_waitcnt vmcnt(0)\n" \
    "v_subrev_u32 v66, 240, v60\n" \
    "buffer_store_dword v65, v66, s[48:51], 0 offen sc1\n" \
    "LO0_HSW%=:\n" \
    "v_subrev_u32 v65, 768, v59\n" \
    "LO0_HSP%=:\n" \
    "buffer_load_dword v64, v65, s[48:51], 0 offen sc1\n" \
    "s_waitcnt vmcnt(0)\n" \
    "v_readlane_b32 s68, v64, 4\n" \
    "v_cmp_eq_u32 vcc, 0, v64\n" \
    "s_and_b32 s55, vcc_lo, 31\n" \
    "s_cmp_eq_u32 s55, 0\n" \
    "s_cbranch_scc1 LO0_HSD%=\n" \
    "s_add_u32 s67, s67, 1\n" \
    "s_cmp_gt_u32 s67, 65536\n" \
    "s_cbranch_scc1 LO0_HSD%=\n" \
    "s_sleep 2\n" \
    "s_branch LO0_HSP%=\n" \
    "LO0_HSD%=:\n" \
    "s_andn2_b32 s68, s68, 3\n" \
    "v_and_b32 v64, 2, v64\n" \
    "v_lshlrev_b32 v64, 12, v64\n" \
    "v_subrev_u32 v53, 1024, v59\n" \
    "v_lshlrev_b32 v53, 18, v53\n" \
    "v_add_u32 v53, v53, v64\n" \
    "s_mov_b32 s67, 0\n" \
    "s_nop 0\n" \
    "v_readlane_b32 s53, v53, 0\n" \
    "v_readlane_b32 s54, v53, 1\n" \
    "v_readlane_b32 s43, v53, 3\n" \
    "v_mov_b32 v158, -1\n" \
    "v_mov_b32 v159, -1\n" \
    "v_mov_b64 v[164:165], 0\n" \
    "v_readfirstlane_b32 s55, v61\n" \
    "s_cmp_lt_u32 s55, 0x10000\n" \
    "s_cselect_b32 s40, 64, 0\n" \
    "s_add_u32 s74, s54, 327680\n" \
    "LO0_LOOP%=:\n" \
    "s_add_u32 s68, s68, %18\n" \
    "s_and_b32 s70, s68, 3\n" \
    "s_andn2_b32 s64, s68, 3\n" \
    "LO0_CWC%=:\n" \
    "s_memrealtime s[72:73]\n" \
    "s_waitcnt lgkmcnt(0)\n" \
    "s_sub_u32 s55, s72, s64\n" \
    "s_cmp_ge_i32 s55, -12\n" \
    "s_cbranch_scc1 LO0_CW%=\n" \
    "s_cmp_lt_i32 s55, -4096\n" \
    "s_cbranch_scc1 LO0_CWO%=\n" \
    "s_sleep 2\n" \
    "s_branch LO0_CWC%=\n" \
    "LO0_CW%=:\n" \
    "s_memrealtime s[72:73]\n" \
    "s_waitcnt lgkmcnt(0)\n" \
    "s_sub_u32 s55, s72, s64\n" \
    "s_cmp_ge_i32 s55, 0\n" \
    "s_cbranch_scc1 LO0_CWO%=\n" \
    "s_memrealtime s[72:73]\n" \
    "s_waitcnt lgkmcnt(0)\n" \
    "s_sub_u32 s55, s72, s64\n" \
    "s_cmp_ge_i32 s55, 0\n" \
    "s_cbranch_scc1 LO0_CWO%=\n" \
    "s_memrealtime s[72:73]\n" \
    "s_waitcnt lgkmcnt(0)\n" \
    "s_sub_u32 s55, s72, s64\n" \
    "s_cmp_ge_i32 s55, 0\n" \
    "s_cbranch_scc1 LO0_CWO%=\n" \
    "s_memrealtime s[72:73]\n" \
    "s_waitcnt lgkmcnt(0)\n" \
    "s_sub_u32 s55, s72, s64\n" \
    "s_cmp_ge_i32 s55, 0\n" \
    "s_cbranch_scc1 LO0_CWO%=\n" \
    "s_branch LO0_CW%=\n" \
    "LO0_CWO%=:\n" \
    "s_mov_b32 s58, s70\n" \
    "LO0_CWD%=:\n" \
    "s_sub_u32 s58, s58, 1\n" \
    "s_cbranch_scc0 LO0_CWD%=\n" \
    "LO0_RETRY%=:\n" \
    "buffer_load_dwordx4 v[64:67], v32, s[44:47], s53 offen sc1\n" \
    "buffer_load_dwordx4 v[68:71], v33, s[44:47], s53 offen sc1\n" \
    "v_mov_b64 v[92:93], 0\n" \
    "v_mov_b64 v[94:95], 0\n" \
    "v_mov_b64 v[96:97], 0\n" \
    "v_mov_b64 v[98:99], 0\n" \
    "v_mov_b64 v[100:101], 0\n" \
    "v_mov_b64 v[102:103], 0\n" \
    "v_mov_b64 v[104:105], 0\n" \
    "v_mov_b64 v[106:107], 0\n" \
    "s_waitcnt vmcnt(1)\n" \
    "ds_write_b128 v34, v[64:67]\n" \
    "v_smfmac_f32_16x16x64_bf16 v[92:95], v[64:67], a[0:7], v52\n" \
    "v_max3_u32 v160, v64, v65, v66\n" \
    "s_waitcnt vmcnt(0)\n" \
    "ds_write_b128 v35, v[68:71]\n" \
    "v_smfmac_f32_16x16x64_bf16 v[96:99], v[64:67], a[56:63], v52\n" \
    "v_max3_u32 v160, v160, v67, v68\n" \
    "v_max3_u32 v160, v160, v69, v70\n" \
    "v_smfmac_f32_16x16x64_bf16 v[100:103], v[64:67], a[112:119], v52\n" \
    "v_max_u32 v160, v160, v71\n" \
    "v_cmp_eq_u32 vcc, -1, v160\n" \
    "v_smfmac_f32_16x16x64_bf16 v[104:107], v[64:67], a[168:175], v52\n" \
    "s_cbranch_vccnz LO0_SLOW%=\n" \
    "LO0_CONT%=:\n" \
    "s_waitcnt lgkmcnt(0)\n" \
    "s_barrier\n" \
    "ds_read_b128 v[72:75], v36\n" \
    "ds_read_b128 v[76:79], v37\n" \
    "ds_read_b128 v[80:83], v38\n" \
    "ds_read_b128 v[84:87], v39\n" \
    "ds_read_b128 v[88:91], v40\n" \
    "ds_read_b64 v[162:163], v56 offset:7168\n" \
    "s_cmp_eq_u32 s52, 0\n" \
    "s_cbranch_scc1 LO0_NODEF%=\n" \
    "v_mov_b32 v147, s52\n" \
    "s_add_u32 s55, s43, 327680\n" \
    "buffer_store_dwordx4 v[124:127], v55, s[48:51], s60 offen sc1\n" \
    "s_cmp_eq_u32 %17, 0\n" \
    "s_cbranch_scc1 LO0_DEF2%=\n" \
    "buffer_store_dword v158, v43, s[44:47], s43 offen sc1\n" \
    "buffer_store_dword v158, v43, s[44:47], s55 offen sc1\n" \
    "LO0_DEF2%=:\n" \
    "s_cmp_eq_u32 s40, 0\n" \
    "s_cbranch_scc1 LO0_DEF3%=\n" \
    "buffer_store_dword v147, v61, s[48:51], 0 offen sc1\n" \
    "buffer_store_dwordx2 v[158:159], v57, s[44:47], s43 offen sc1\n" \
    "LO0_DEF3%=:\n" \
    "s_add_u32 s60, s60, 114688\n" \
    "s_add_u32 s42, s42, 8192\n" \
    "s_add_u32 s55, s52, 1\n" \
    "s_and_b32 s55, s55, 3\n" \
    "s_xor_b32 s64, s55, 2\n" \
    "v_readlane_b32 s54, v53, s55\n" \
    "v_readlane_b32 s43, v53, s64\n" \
    "s_nop 0\n" \
    "s_add_u32 s74, s54, 327680\n" \
    "LO0_NODEF%=:\n" \
    "s_cmp_ge_u32 s41, s40\n" \
    "s_cbranch_scc1 LO0_NOFG%=\n" \
    "buffer_load_dword v164, v61, s[48:51], 0 offen offset:256 sc1\n" \
    "LO0_NOFG%=:\n" \
    "v_smfmac_f32_16x16x64_bf16 v[92:95], v[68:71], a[8:15], v52\n" \
    "v_smfmac_f32_16x16x64_bf16 v[96:99], v[68:71], a[64:71], v52\n" \
    "v_smfmac_f32_16x16x64_bf16 v[100:103], v[68:71], a[120:127], v52\n" \
    "v_smfmac_f32_16x16x64_bf16 v[104:107], v[68:71], a[176:183], v52\n" \
    "s_waitcnt lgkmcnt(5)\n" \
    "v_smfmac_f32_16x16x64_bf16 v[92:95], v[72:75], a[16:23], v52\n" \
    "s_waitcnt lgkmcnt(4)\n" \
    "v_smfmac_f32_16x16x64_bf16 v[92:95], v[76:79], a[24:31], v52\n" \
    "s_waitcnt lgkmcnt(3)\n" \
    "v_smfmac_f32_16x16x64_bf16 v[92:95], v[80:83], a[32:39], v52\n" \
    "s_waitcnt lgkmcnt(2)\n" \
    "v_smfmac_f32_16x16x64_bf16 v[92:95], v[84:87], a[40:47], v52\n" \
    "s_waitcnt lgkmcnt(1)\n" \
    "v_smfmac_f32_16x16x64_bf16 v[92:95], v[88:91], a[48:55], v52\n" \
    "v_smfmac_f32_16x16x64_bf16 v[96:99], v[72:75], a[72:79], v52\n" \
    "v_smfmac_f32_16x16x64_bf16 v[96:99], v[76:79], a[80:87], v52\n" \
    "v_smfmac_f32_16x16x64_bf16 v[96:99], v[80:83], a[88:95], v52\n" \
    "v_smfmac_f32_16x16x64_bf16 v[96:99], v[84:87], a[96:103], v52\n" \
    "v_smfmac_f32_16x16x64_bf16 v[96:99], v[88:91], a[104:111], v52\n" \
    "v_smfmac_f32_16x16x64_bf16 v[100:103], v[72:75], a[128:135], v52\n" \
    "s_nop 2\n" \
    "v_smfmac_f32_16x16x64_bf16 v[100:103], v[76:79], a[136:143], v52\n" \
    "s_nop 2\n" \
    "v_smfmac_f32_16x16x64_bf16 v[100:103], v[80:83], a[144:151], v52\n" \
    "v_permlane32_swap_b32 v92, v96\n" \
    "v_permlane32_swap_b32 v93, v97\n" \
    "v_smfmac_f32_16x16x64_bf16 v[100:103], v[84:87], a[152:159], v52\n" \
    "v_permlane32_swap_b32 v94, v98\n" \
    "v_permlane32_swap_b32 v95, v99\n" \
    "v_smfmac_f32_16x16x64_bf16 v[100:103], v[88:91], a[160:167], v52\n" \
    "v_pk_add_f32 v[144:145], v[92:93], v[96:97]\n" \
    "v_pk_add_f32 v[146:147], v[94:95], v[98:99]\n" \
    "v_smfmac_f32_16x16x64_bf16 v[104:107], v[72:75], a[184:191], v52\n" \
    "v_cvt_pk_bf16_f32 v124, v144, v145\n" \
    "v_cvt_pk_bf16_f32 v125, v146, v147\n" \
    "v_smfmac_f32_16x16x64_bf16 v[104:107], v[76:79], a[192:199], v52\n" \
    "ds_write_b64 v44, v[124:125]\n" \
    "v_smfmac_f32_16x16x64_bf16 v[104:107], v[80:83], a[200:207], v52\n" \
    "v_smfmac_f32_16x16x64_bf16 v[104:107], v[84:87], a[208:215], v52\n" \
    "v_smfmac_f32_16x16x64_bf16 v[104:107], v[88:91], a[216:223], v52\n" \
    "s_nop 7\n" \
    "v_permlane32_swap_b32 v100, v104\n" \
    "v_permlane32_swap_b32 v101, v105\n" \
    "v_permlane32_swap_b32 v102, v106\n" \
    "v_permlane32_swap_b32 v103, v107\n" \
    "v_pk_add_f32 v[144:145], v[100:101], v[104:105]\n" \
    "v_pk_add_f32 v[146:147], v[102:103], v[106:107]\n" \
    "v_cvt_pk_bf16_f32 v126, v144, v145\n" \
    "v_cvt_pk_bf16_f32 v127, v146, v147\n" \
    "ds_write_b64 v44, v[126:127] offset:512\n" \
    "s_cmp_eq_u32 s52, 63\n" \
    "s_cbranch_scc1 LO0_LAST%=\n" \
    "ds_read_b128 v[128:131], v45\n" \
    "s_mov_b32 s58, s62\n" \
    "LO0_DS%=:\n" \
    "s_sub_u32 s58, s58, 1\n" \
    "s_cbranch_scc0 LO0_DS%=\n" \
    "s_waitcnt vmcnt(0)\n" \
    "buffer_load_dwordx2 v[108:109], v42, s[44:47], s53 offen offset:0 sc1\n" \
    "buffer_load_dwordx2 v[110:111], v42, s[44:47], s53 offen offset:128 sc1\n" \
    "buffer_load_dwordx2 v[112:113], v42, s[44:47], s53 offen offset:256 sc1\n" \
    "buffer_load_dwordx2 v[114:115], v42, s[44:47], s53 offen offset:384 sc1\n" \
    "buffer_load_dword v120, v48, s[44:47], s53 offen sc1\n" \
    "buffer_load_dwordx2 v[116:117], v41, s[44:47], s53 offen sc1\n" \
    "buffer_load_dwordx2 v[118:119], v41, s[44:47], s53 offen offset:128 sc1\n" \
    "s_sleep 2\n" \
    "buffer_load_dwordx2 v[168:169], v42, s[44:47], s53 offen offset:0 sc1\n" \
    "buffer_load_dwordx2 v[170:171], v42, s[44:47], s53 offen offset:128 sc1\n" \
    "buffer_load_dwordx2 v[172:173], v42, s[44:47], s53 offen offset:256 sc1\n" \
    "buffer_load_dwordx2 v[174:175], v42, s[44:47], s53 offen offset:384 sc1\n" \
    "buffer_load_dword v180, v48, s[44:47], s53 offen sc1\n" \
    "buffer_load_dwordx2 v[176:177], v41, s[44:47], s53 offen sc1\n" \
    "buffer_load_dwordx2 v[178:179], v41, s[44:47], s53 offen offset:128 sc1\n" \
    "s_mov_b32 s66, 0\n" \
    "s_waitcnt lgkmcnt(0)\n" \
    "v_readfirstlane_b32 s57, v162\n" \
    "s_waitcnt vmcnt(9)\n" \
    "LO0_MP%=:\n" \
    "v_pk_add_f32 v[144:145], v[108:109], v[110:111]\n" \
    "v_pk_add_f32 v[146:147], v[112:113], v[114:115]\n" \
    "v_min_f32 v155, v120, v49\n" \
    "v_pk_add_f32 v[144:145], v[144:145], v[146:147]\n" \
    "v_max3_f32 v148, v144, v145, v155\n" \
    "v_max3_u32 v160, v108, v109, v110\n" \
    "v_max3_u32 v160, v160, v111, v112\n" \
    "v_max_f32_dpp v148, v148, v148 quad_perm:[1,0,3,2] row_mask:0xf bank_mask:0xf\n" \
    "v_max3_u32 v160, v160, v113, v114\n" \
    "v_max3_u32 v160, v160, v115, v120\n" \
    "v_max_f32_dpp v148, v148, v148 quad_perm:[2,3,0,1] row_mask:0xf bank_mask:0xf\n" \
    "v_sub_f32 v149, v155, v148\n" \
    "v_pk_add_f32 v[150:151], v[144:145], v[148:149] op_sel_hi:[1,0] neg_lo:[0,1] neg_hi:[0,1]\n" \
    "v_exp_f32 v149, v149\n" \
    "v_exp_f32 v150, v150\n" \
    "v_exp_f32 v151, v151\n" \
    "v_mul_f32_dpp v152, v120, v149 quad_perm:[2,3,2,3] row_mask:0xf bank_mask:0xf\n" \
    "v_add_f32 v146, v150, v151\n" \
    "v_add_f32 v152, v152, v146\n" \
    "s_waitcnt vmcnt(7)\n" \
    "v_max3_u32 v160, v160, v116, v117\n" \
    "v_add_f32_dpp v152, v152, v152 quad_perm:[1,0,3,2] row_mask:0xf bank_mask:0xf\n" \
    "v_max3_u32 v160, v160, v118, v119\n" \
    "v_cmp_eq_u32 vcc, -1, v160\n" \
    "v_add_f32_dpp v152, v152, v152 quad_perm:[2,3,0,1] row_mask:0xf bank_mask:0xf\n" \
    "v_rcp_f32 v152, v152\n" \
    "s_cbranch_vccnz LO0_SLOWS%=\n" \
    "LO0_CONTS%=:\n" \
    "v_pk_mul_f32 v[150:151], v[150:151], v[152:153] op_sel_hi:[1,0]\n" \
    "v_mul_f32 v149, v149, v152\n" \
    "v_cvt_pk_bf16_f32 v153, v150, v151\n" \
    "v_cvt_pk_bf16_f32 v154, v149, v149\n" \
    "s_memrealtime s[72:73]\n" \
    "v_mov_b32_dpp v136, v153 quad_perm:[0,0,0,0] row_mask:0xf bank_mask:0xf\n" \
    "v_mov_b32_dpp v137, v153 quad_perm:[1,1,1,1] row_mask:0xf bank_mask:0xf\n" \
    "v_mov_b32_dpp v138, v153 quad_perm:[2,2,2,2] row_mask:0xf bank_mask:0xf\n" \
    "v_mov_b32_dpp v139, v153 quad_perm:[3,3,3,3] row_mask:0xf bank_mask:0xf\n" \
    "v_and_b32_dpp v140, v154, v50 quad_perm:[0,0,0,0] row_mask:0xf bank_mask:0xf\n" \
    "v_and_b32_dpp v141, v154, v51 quad_perm:[0,0,0,0] row_mask:0xf bank_mask:0xf\n" \
    "v_mfma_f32_16x16x32_bf16 v[132:135], v[136:139], v[128:131], 0\n" \
    "v_and_b32_dpp v142, v154, v50 quad_perm:[1,1,1,1] row_mask:0xf bank_mask:0xf\n" \
    "v_and_b32_dpp v143, v154, v51 quad_perm:[1,1,1,1] row_mask:0xf bank_mask:0xf\n" \
    "s_nop 1\n" \
    "v_mfma_f32_16x16x32_bf16 v[132:135], v[140:143], v[116:119], v[132:135]\n" \
    "s_cmp_eq_u32 %17, 0\n" \
    "s_nop 6\n" \
    "ds_write_b32 v46, v132\n" \
    "s_waitcnt lgkmcnt(0)\n" \
    "s_barrier\n" \
    "s_cbranch_scc1 LO0_NOFINS%=\n" \
    "ds_read_b64 v[144:145], v47 offset:0\n" \
    "ds_read_b64 v[146:147], v47 offset:256\n" \
    "ds_read_b64 v[148:149], v47 offset:512\n" \
    "ds_read_b64 v[150:151], v47 offset:768\n" \
    "s_waitcnt lgkmcnt(2)\n" \
    "v_pk_add_f32 v[144:145], v[144:145], v[146:147]\n" \
    "s_waitcnt lgkmcnt(0)\n" \
    "v_pk_add_f32 v[148:149], v[148:149], v[150:151]\n" \
    "v_pk_add_f32 v[144:145], v[144:145], v[148:149]\n" \
    "v_cvt_pk_bf16_f32 v157, v144, v145\n" \
    "v_min_u32 v157, -2, v157\n" \
    "buffer_store_dword v157, v43, s[44:47], s54 offen sc1\n" \
    "buffer_store_dword v157, v43, s[44:47], s74 offen sc1\n" \
    "LO0_NOFIN%=:\n" \
    "s_mov_b32 s63, 0\n" \
    "s_mov_b32 s68, s57\n" \
    "s_add_u32 s52, s52, 1\n" \
    "s_mov_b32 s53, s54\n" \
    "s_branch LO0_LOOP%=\n" \
    "LO0_SLOW%=:\n" \
    "s_add_u32 s65, s65, 1\n" \
    "s_cmp_gt_u32 s65, 65536\n" \
    "s_cbranch_scc1 LO0_CONT%=\n" \
    "s_mov_b32 s63, 1\n" \
    "s_nop 7\n" \
    "s_branch LO0_RETRY%=\n" \
    "LO0_SLOWS%=:\n" \
    "s_cmp_lg_u32 s66, 0\n" \
    "s_cbranch_scc1 LO0_SLOWS2%=\n" \
    "s_mov_b32 s66, 1\n" \
    "s_waitcnt vmcnt(0)\n" \
    "v_mov_b64 v[108:109], v[168:169]\n" \
    "v_mov_b64 v[110:111], v[170:171]\n" \
    "v_mov_b64 v[112:113], v[172:173]\n" \
    "v_mov_b64 v[114:115], v[174:175]\n" \
    "v_mov_b64 v[116:117], v[176:177]\n" \
    "v_mov_b64 v[118:119], v[178:179]\n" \
    "v_mov_b64 v[120:121], v[180:181]\n" \
    "s_branch LO0_MP%=\n" \
    "LO0_SLOWS2%=:\n" \
    "s_mov_b32 s63, 1\n" \
    "s_add_u32 s67, s67, 1\n" \
    "s_cmp_gt_u32 s67, 65536\n" \
    "s_cbranch_scc1 LO0_CONTS%=\n" \
    "buffer_load_dwordx2 v[108:109], v42, s[44:47], s53 offen offset:0 sc1\n" \
    "buffer_load_dwordx2 v[110:111], v42, s[44:47], s53 offen offset:128 sc1\n" \
    "buffer_load_dwordx2 v[112:113], v42, s[44:47], s53 offen offset:256 sc1\n" \
    "buffer_load_dwordx2 v[114:115], v42, s[44:47], s53 offen offset:384 sc1\n" \
    "buffer_load_dword v120, v48, s[44:47], s53 offen sc1\n" \
    "buffer_load_dwordx2 v[116:117], v41, s[44:47], s53 offen sc1\n" \
    "buffer_load_dwordx2 v[118:119], v41, s[44:47], s53 offen offset:128 sc1\n" \
    "s_waitcnt vmcnt(0)\n" \
    "s_branch LO0_MP%=\n" \
    "LO0_NOFINS%=:\n" \
    "s_cmp_eq_u32 s40, 0\n" \
    "s_cbranch_scc1 LO0_NOANN%=\n" \
    "s_add_u32 s55, s57, s69\n" \
    "s_add_u32 s55, s55, s71\n" \
    "s_and_b32 s64, s57, 3\n" \
    "s_add_u32 s64, s64, s71\n" \
    "s_cmp_ge_u32 s64, 3\n" \
    "s_cselect_b32 s64, 1, 0\n" \
    "s_add_u32 s55, s55, s64\n" \
    "s_sub_u32 s64, s57, s72\n" \
    "s_sub_u32 s58, s64, 38\n" \
    "s_cmp_lt_u32 s58, 14\n" \
    "s_cselect_b32 s58, -4, 0\n" \
    "s_add_u32 s69, s69, s58\n" \
    "s_max_u32 s69, s69, %10\n" \
    "s_cmp_lt_i32 s64, 8\n" \
    "s_cselect_b32 s63, 1, s63\n" \
    "s_add_u32 s64, s72, s56\n" \
    "s_andn2_b32 s64, s64, 3\n" \
    "s_cmp_lg_u32 s63, 0\n" \
    "s_cselect_b32 s55, s64, s55\n" \
    "s_cselect_b32 s64, 1, 0\n" \
    "s_cmp_gt_u32 s52, 5\n" \
    "s_cselect_b32 s64, s64, 0\n" \
    "s_add_u32 s61, s61, s64\n" \
    "s_and_b32 s64, s64, s61\n" \
    "s_lshl_b32 s64, s64, 2\n" \
    "s_add_u32 s69, s69, s64\n" \
    "v_mov_b32 v146, s55\n" \
    "v_readfirstlane_b32 s41, v164\n" \
    "s_min_u32 s41, s41, 64\n" \
    "v_mov_b32 v147, s41\n" \
    "buffer_store_dwordx2 v[146:147], v57, s[44:47], s54 offen sc1\n" \
    "s_branch LO0_NOFIN%=\n" \
    "LO0_NOANN%=:\n" \
    "s_sleep 3\n" \
    "s_branch LO0_NOFIN%=\n" \
    "LO0_LAST%=:\n" \
    "buffer_store_dwordx4 v[124:127], v55, s[48:51], s60 offen sc1\n" \
    "s_waitcnt vmcnt(0) lgkmcnt(0)\n"
#define ASM_OUT1 \
    "s_mov_b32 s44, %1\n" \
    "s_mov_b32 s45, %2\n" \
    "s_mov_b32 s46, %3\n" \
    "s_mov_b32 s47, %4\n" \
    "s_mov_b32 s48, %5\n" \
    "s_mov_b32 s49, %6\n" \
    "s_mov_b32 s50, %7\n" \
    "s_mov_b32 s51, %8\n" \
    "ds_read_b32 v32, %0 offset:0\n" \
    "ds_read_b32 v33, %0 offset:256\n" \
    "ds_read_b32 v34, %0 offset:512\n" \
    "ds_read_b32 v35, %0 offset:768\n" \
    "ds_read_b32 v36, %0 offset:1024\n" \
    "ds_read_b32 v37, %0 offset:1280\n" \
    "ds_read_b32 v38, %0 offset:1536\n" \
    "ds_read_b32 v39, %0 offset:1792\n" \
    "ds_read_b32 v40, %0 offset:2048\n" \
    "ds_read_b32 v41, %0 offset:2304\n" \
    "ds_read_b32 v42, %0 offset:2560\n" \
    "ds_read_b32 v43, %0 offset:2816\n" \
    "s_waitcnt lgkmcnt(0)\n" \
    "ds_read_b32 v44, %0 offset:3072\n" \
    "ds_read_b32 v45, %0 offset:3328\n" \
    "ds_read_b32 v46, %0 offset:3584\n" \
    "ds_read_b32 v47, %0 offset:3840\n" \
    "ds_read_b32 v48, %0 offset:4096\n" \
    "ds_read_b32 v49, %0 offset:4352\n" \
    "ds_read_b32 v50, %0 offset:4608\n" \
    "ds_read_b32 v51, %0 offset:4864\n" \
    "ds_read_b32 v52, %0 offset:5120\n" \
    "ds_read_b32 v53, %0 offset:5376\n" \
    "ds_read_b32 v54, %0 offset:5632\n" \
    "ds_read_b32 v55, %0 offset:5888\n" \
    "s_waitcnt lgkmcnt(0)\n" \
    "ds_read_b32 v56, %0 offset:6144\n" \
    "ds_read_b32 v57, %0 offset:6400\n" \
    "ds_read_b32 v58, %0 offset:6656\n" \
    "ds_read_b32 v59, %0 offset:6912\n" \
    "ds_read_b32 v60, %0 offset:7168\n" \
    "ds_read_b32 v61, %0 offset:7424\n" \
    "s_mov_b32 s41, 0\n" \
    "s_mov_b32 s40, 0\n" \
    "s_mov_b32 s69, %10\n" \
    "s_mov_b32 s71, %11\n" \
    "s_mov_b32 s56, %12\n" \
    "s_mov_b32 s63, 0\n" \
    "s_mov_b32 s52, 0\n" \
    "s_mov_b32 s65, 0\n" \
    "s_mov_b32 s66, 0\n" \
    "s_mov_b32 s67, 0\n" \
    "s_waitcnt lgkmcnt(0)\n" \
    "s_mov_b32 s60, %16\n" \
    "s_mov_b32 s62, %15\n" \
    "s_mov_b32 s42, %19\n" \
    "s_mov_b32 s61, 0\n" \
    "s_mov_b32 s58, %13\n" \
    "s_lshl_b32 s55, s58, 11\n" \
    "s_add_u32 s64, s55, %14\n" \
    "buffer_load_dwordx4 a[0:3], v54, s[48:51], s64 offen\n" \
    "buffer_load_dwordx4 a[4:7], v54, s[48:51], s64 offen offset:1024\n" \
    "s_add_u32 s64, s64, 401408\n" \
    "buffer_load_dwordx4 a[56:59], v54, s[48:51], s64 offen\n" \
    "buffer_load_dwordx4 a[60:63], v54, s[48:51], s64 offen offset:1024\n" \
    "s_add_u32 s64, s64, 401408\n" \
    "buffer_load_dwordx4 a[112:115], v54, s[48:51], s64 offen\n" \
    "buffer_load_dwordx4 a[116:119], v54, s[48:51], s64 offen offset:1024\n" \
    "s_add_u32 s64, s64, 401408\n" \
    "buffer_load_dwordx4 a[168:171], v54, s[48:51], s64 offen\n" \
    "buffer_load_dwordx4 a[172:175], v54, s[48:51], s64 offen offset:1024\n" \
    "s_add_u32 s58, s58, 1\n" \
    "s_cmp_eq_u32 s58, 7\n" \
    "s_cselect_b32 s58, 0, s58\n" \
    "s_lshl_b32 s55, s58, 11\n" \
    "s_add_u32 s64, s55, %14\n" \
    "buffer_load_dwordx4 a[8:11], v54, s[48:51], s64 offen\n" \
    "buffer_load_dwordx4 a[12:15], v54, s[48:51], s64 offen offset:1024\n" \
    "s_add_u32 s64, s64, 401408\n" \
    "buffer_load_dwordx4 a[64:67], v54, s[48:51], s64 offen\n" \
    "buffer_load_dwordx4 a[68:71], v54, s[48:51], s64 offen offset:1024\n" \
    "s_add_u32 s64, s64, 401408\n" \
    "buffer_load_dwordx4 a[120:123], v54, s[48:51], s64 offen\n" \
    "buffer_load_dwordx4 a[124:127], v54, s[48:51], s64 offen offset:1024\n" \
    "s_add_u32 s64, s64, 401408\n" \
    "buffer_load_dwordx4 a[176:179], v54, s[48:51], s64 offen\n" \
    "buffer_load_dwordx4 a[180:183], v54, s[48:51], s64 offen offset:1024\n" \
    "s_add_u32 s58, s58, 1\n" \
    "s_cmp_eq_u32 s58, 7\n" \
    "s_cselect_b32 s58, 0, s58\n" \
    "s_lshl_b32 s55, s58, 11\n" \
    "s_add_u32 s64, s55, %14\n" \
    "buffer_load_dwordx4 a[16:19], v54, s[48:51], s64 offen\n" \
    "buffer_load_dwordx4 a[20:23], v54, s[48:51], s64 offen offset:1024\n" \
    "s_add_u32 s64, s64, 401408\n" \
    "buffer_load_dwordx4 a[72:75], v54, s[48:51], s64 offen\n" \
    "buffer_load_dwordx4 a[76:79], v54, s[48:51], s64 offen offset:1024\n" \
    "s_add_u32 s64, s64, 401408\n" \
    "buffer_load_dwordx4 a[128:131], v54, s[48:51], s64 offen\n" \
    "buffer_load_dwordx4 a[132:135], v54, s[48:51], s64 offen offset:1024\n" \
    "s_add_u32 s64, s64, 401408\n" \
    "buffer_load_dwordx4 a[184:187], v54, s[48:51], s64 offen\n" \
    "buffer_load_dwordx4 a[188:191], v54, s[48:51], s64 offen offset:1024\n" \
    "s_add_u32 s58, s58, 1\n" \
    "s_cmp_eq_u32 s58, 7\n" \
    "s_cselect_b32 s58, 0, s58\n" \
    "s_lshl_b32 s55, s58, 11\n" \
    "s_add_u32 s64, s55, %14\n" \
    "buffer_load_dwordx4 a[24:27], v54, s[48:51], s64 offen\n" \
    "buffer_load_dwordx4 a[28:31], v54, s[48:51], s64 offen offset:1024\n" \
    "s_add_u32 s64, s64, 401408\n" \
    "buffer_load_dwordx4 a[80:83], v54, s[48:51], s64 offen\n" \
    "buffer_load_dwordx4 a[84:87], v54, s[48:51], s64 offen offset:1024\n" \
    "s_add_u32 s64, s64, 401408\n" \
    "buffer_load_dwordx4 a[136:139], v54, s[48:51], s64 offen\n" \
    "buffer_load_dwordx4 a[140:143], v54, s[48:51], s64 offen offset:1024\n" \
    "s_add_u32 s64, s64, 401408\n" \
    "buffer_load_dwordx4 a[192:195], v54, s[48:51], s64 offen\n" \
    "buffer_load_dwordx4 a[196:199], v54, s[48:51], s64 offen offset:1024\n" \
    "s_add_u32 s58, s58, 1\n" \
    "s_cmp_eq_u32 s58, 7\n" \
    "s_cselect_b32 s58, 0, s58\n" \
    "s_waitcnt vmcnt(8)\n" \
    "s_lshl_b32 s55, s58, 11\n" \
    "s_add_u32 s64, s55, %14\n" \
    "buffer_load_dwordx4 a[32:35], v54, s[48:51], s64 offen\n" \
    "buffer_load_dwordx4 a[36:39], v54, s[48:51], s64 offen offset:1024\n" \
    "s_add_u32 s64, s64, 401408\n" \
    "buffer_load_dwordx4 a[88:91], v54, s[48:51], s64 offen\n" \
    "buffer_load_dwordx4 a[92:95], v54, s[48:51], s64 offen offset:1024\n" \
    "s_add_u32 s64, s64, 401408\n" \
    "buffer_load_dwordx4 a[144:147], v54, s[48:51], s64 offen\n" \
    "buffer_load_dwordx4 a[148:151], v54, s[48:51], s64 offen offset:1024\n" \
    "s_add_u32 s64, s64, 401408\n" \
    "buffer_load_dwordx4 a[200:203], v54, s[48:51], s64 offen\n" \
    "buffer_load_dwordx4 a[204:207], v54, s[48:51], s64 offen offset:1024\n" \
    "s_add_u32 s58, s58, 1\n" \
    "s_cmp_eq_u32 s58, 7\n" \
    "s_cselect_b32 s58, 0, s58\n" \
    "s_lshl_b32 s55, s58, 11\n" \
    "s_add_u32 s64, s55, %14\n" \
    "buffer_load_dwordx4 a[40:43], v54, s[48:51], s64 offen\n" \
    "buffer_load_dwordx4 a[44:47], v54, s[48:51], s64 offen offset:1024\n" \
    "s_add_u32 s64, s64, 401408\n" \
    "buffer_load_dwordx4 a[96:99], v54, s[48:51], s64 offen\n" \
    "buffer_load_dwordx4 a[100:103], v54, s[48:51], s64 offen offset:1024\n" \
    "s_add_u32 s64, s64, 401408\n" \
    "buffer_load_dwordx4 a[152:155], v54, s[48:51], s64 offen\n" \
    "buffer_load_dwordx4 a[156:159], v54, s[48:51], s64 offen offset:1024\n" \
    "s_add_u32 s64, s64, 401408\n" \
    "buffer_load_dwordx4 a[208:211], v54, s[48:51], s64 offen\n" \
    "buffer_load_dwordx4 a[212:215], v54, s[48:51], s64 offen offset:1024\n" \
    "s_add_u32 s58, s58, 1\n" \
    "s_cmp_eq_u32 s58, 7\n" \
    "s_cselect_b32 s58, 0, s58\n" \
    "s_lshl_b32 s55, s58, 11\n" \
    "s_add_u32 s64, s55, %14\n" \
    "buffer_load_dwordx4 a[48:51], v54, s[48:51], s64 offen\n" \
    "buffer_load_dwordx4 a[52:55], v54, s[48:51], s64 offen offset:1024\n" \
    "s_add_u32 s64, s64, 401408\n" \
    "buffer_load_dwordx4 a[104:107], v54, s[48:51], s64 offen\n" \
    "buffer_load_dwordx4 a[108:111], v54, s[48:51], s64 offen offset:1024\n" \
    "s_add_u32 s64, s64, 401408\n" \
    "buffer_load_dwordx4 a[160:163], v54, s[48:51], s64 offen\n" \
    "buffer_load_dwordx4 a[164:167], v54, s[48:51], s64 offen offset:1024\n" \
    "s_add_u32 s64, s64, 401408\n" \
    "buffer_load_dwordx4 a[216:219], v54, s[48:51], s64 offen\n" \
    "buffer_load_dwordx4 a[220:223], v54, s[48:51], s64 offen offset:1024\n" \
    "s_add_u32 s58, s58, 1\n" \
    "s_cmp_eq_u32 s58, 7\n" \
    "s_cselect_b32 s58, 0, s58\n" \
    "s_waitcnt vmcnt(0)\n" \
    "s_barrier\n" \
    "v_mov_b32 v64, 1\n" \
    "buffer_atomic_add v64, v60, s[48:51], 0 offen sc0\n" \
    "s_waitcnt vmcnt(0)\n" \
    "v_readfirstlane_b32 s55, v64\n" \
    "s_cmp_lg_u32 s55, 119\n" \
    "s_cbranch_scc1 LO1_HSW%=\n" \
    "s_memrealtime s[72:73]\n" \
    "s_waitcnt lgkmcnt(0)\n" \
    "s_add_u32 s55, s72, %9\n" \
    "s_andn2_b32 s55, s55, 3\n" \
    "s_add_u32 s64, s55, s56\n" \
    "v_mov_b32 v68, s64\n" \
    "v_mov_b32 v69, 0\n" \
    "v_subrev_u32 v65, 1024, v59\n" \
    "v_lshrrev_b32 v66, 3, v65\n" \
    "v_mul_u32_u24 v66, 163840, v66\n" \
    "v_and_b32 v67, 4, v65\n" \
    "v_lshlrev_b32 v67, 11, v67\n" \
    "v_add3_u32 v66, v66, v67, v67\n" \
    "v_sub_u32 v66, v66, v67\n" \
    "v_add_u32 v66, 7168, v66\n" \
    "v_mov_b32 v67, 0x7fffff00\n" \
    "v_cmp_gt_u32 vcc, 32, v65\n" \
    "s_nop 1\n" \
    "v_cndmask_b32 v66, v67, v66, vcc\n" \
    "buffer_store_dwordx2 v[68:69], v66, s[44:47], 0 offen sc1\n" \
    "s_or_b32 s55, s55, 1\n" \
    "v_mov_b32 v65, s55\n" \
    "s_waitcnt vmcnt(0)\n" \
    "v_subrev_u32 v66, 240, v60\n" \
    "buffer_store_dword v65, v66, s[48:51], 0 offen sc1\n" \
    "LO1_HSW%=:\n" \
    "v_subrev_u32 v65, 768, v59\n" \
    "LO1_HSP%=:\n" \
    "buffer_load_dword v64, v65, s[48:51], 0 offen sc1\n" \
    "s_waitcnt vmcnt(0)\n" \
    "v_readlane_b32 s68, v64, 4\n" \
    "v_cmp_eq_u32 vcc, 0, v64\n" \
    "s_and_b32 s55, vcc_lo, 31\n" \
    "s_cmp_eq_u32 s55, 0\n" \
    "s_cbranch_scc1 LO1_HSD%=\n" \
    "s_add_u32 s67, s67, 1\n" \
    "s_cmp_gt_u32 s67, 65536\n" \
    "s_cbranch_scc1 LO1_HSD%=\n" \
    "s_sleep 2\n" \
    "s_branch LO1_HSP%=\n" \
    "LO1_HSD%=:\n" \
    "s_andn2_b32 s68, s68, 3\n" \
    "v_and_b32 v64, 2, v64\n" \
    "v_lshlrev_b32 v64, 12, v64\n" \
    "v_subrev_u32 v53, 1024, v59\n" \
    "v_lshlrev_b32 v53, 18, v53\n" \
    "v_add_u32 v53, v53, v64\n" \
    "s_mov_b32 s67, 0\n" \
    "s_nop 0\n" \
    "v_readlane_b32 s53, v53, 0\n" \
    "v_readlane_b32 s54, v53, 1\n" \
    "v_readlane_b32 s43, v53, 3\n" \
    "v_mov_b32 v158, -1\n" \
    "v_mov_b32 v159, -1\n" \
    "v_mov_b64 v[164:165], 0\n" \
    "v_readfirstlane_b32 s55, v61\n" \
    "s_cmp_lt_u32 s55, 0x10000\n" \
    "s_cselect_b32 s40, 64, 0\n" \
    "s_add_u32 s74, s54, 327680\n" \
    "LO1_LOOP%=:\n" \
    "s_add_u32 s68, s68, %18\n" \
    "s_and_b32 s70, s68, 3\n" \
    "s_andn2_b32 s64, s68, 3\n" \
    "LO1_CWC%=:\n" \
    "s_memrealtime s[72:73]\n" \
    "s_waitcnt lgkmcnt(0)\n" \
    "s_sub_u32 s55, s72, s64\n" \
    "s_cmp_ge_i32 s55, -12\n" \
    "s_cbranch_scc1 LO1_CW%=\n" \
    "s_cmp_lt_i32 s55, -4096\n" \
    "s_cbranch_scc1 LO1_CWO%=\n" \
    "s_sleep 2\n" \
    "s_branch LO1_CWC%=\n" \
    "LO1_CW%=:\n" \
    "s_memrealtime s[72:73]\n" \
    "s_waitcnt lgkmcnt(0)\n" \
    "s_sub_u32 s55, s72, s64\n" \
    "s_cmp_ge_i32 s55, 0\n" \
    "s_cbranch_scc1 LO1_CWO%=\n" \
    "s_memrealtime s[72:73]\n" \
    "s_waitcnt lgkmcnt(0)\n" \
    "s_sub_u32 s55, s72, s64\n" \
    "s_cmp_ge_i32 s55, 0\n" \
    "s_cbranch_scc1 LO1_CWO%=\n" \
    "s_memrealtime s[72:73]\n" \
    "s_waitcnt lgkmcnt(0)\n" \
    "s_sub_u32 s55, s72, s64\n" \
    "s_cmp_ge_i32 s55, 0\n" \
    "s_cbranch_scc1 LO1_CWO%=\n" \
    "s_memrealtime s[72:73]\n" \
    "s_waitcnt lgkmcnt(0)\n" \
    "s_sub_u32 s55, s72, s64\n" \
    "s_cmp_ge_i32 s55, 0\n" \
    "s_cbranch_scc1 LO1_CWO%=\n" \
    "s_branch LO1_CW%=\n" \
    "LO1_CWO%=:\n" \
    "s_mov_b32 s58, s70\n" \
    "LO1_CWD%=:\n" \
    "s_sub_u32 s58, s58, 1\n" \
    "s_cbranch_scc0 LO1_CWD%=\n" \
    "LO1_RETRY%=:\n" \
    "buffer_load_dwordx4 v[64:67], v32, s[44:47], s53 offen sc1\n" \
    "buffer_load_dwordx2 v[162:163], v33, s[44:47], s53 offen sc1\n" \
    "v_mov_b64 v[92:93], 0\n" \
    "v_mov_b64 v[94:95], 0\n" \
    "v_mov_b64 v[96:97], 0\n" \
    "v_mov_b64 v[98:99], 0\n" \
    "v_mov_b64 v[100:101], 0\n" \
    "v_mov_b64 v[102:103], 0\n" \
    "v_mov_b64 v[104:105], 0\n" \
    "v_mov_b64 v[106:107], 0\n" \
    "s_waitcnt vmcnt(1)\n" \
    "ds_write_b128 v34, v[64:67]\n" \
    "v_smfmac_f32_16x16x64_bf16 v[92:95], v[64:67], a[0:7], v52\n" \
    "v_max3_u32 v160, v64, v65, v66\n" \
    "s_waitcnt vmcnt(0)\n" \
    "ds_write_b64 v34, v[162:163] offset:1024\n" \
    "v_smfmac_f32_16x16x64_bf16 v[96:99], v[64:67], a[56:63], v52\n" \
    "v_max3_u32 v160, v160, v67, v162\n" \
    "v_max_u32 v160, v160, v163\n" \
    "v_smfmac_f32_16x16x64_bf16 v[100:103], v[64:67], a[112:119], v52\n" \
    "v_cmp_eq_u32 vcc, -1, v160\n" \
    "v_smfmac_f32_16x16x64_bf16 v[104:107], v[64:67], a[168:175], v52\n" \
    "s_cbranch_vccnz LO1_SLOW%=\n" \
    "LO1_CONT%=:\n" \
    "s_waitcnt lgkmcnt(0)\n" \
    "s_barrier\n" \
    "ds_read_b128 v[68:71], v35\n" \
    "ds_read_b128 v[72:75], v36\n" \
    "ds_read_b128 v[76:79], v37\n" \
    "ds_read_b128 v[80:83], v38\n" \
    "ds_read_b128 v[84:87], v39\n" \
    "ds_read_b128 v[88:91], v40\n" \
    "s_cmp_eq_u32 s52, 0\n" \
    "s_cbranch_scc1 LO1_NODEF%=\n" \
    "v_mov_b32 v147, s52\n" \
    "s_add_u32 s55, s43, 327680\n" \
    "buffer_store_dwordx4 v[124:127], v55, s[48:51], s60 offen sc1\n" \
    "s_cmp_eq_u32 %17, 0\n" \
    "s_cbranch_scc1 LO1_DEF2%=\n" \
    "buffer_store_dword v158, v43, s[44:47], s43 offen sc1\n" \
    "buffer_store_dword v158, v43, s[44:47], s55 offen sc1\n" \
    "LO1_DEF2%=:\n" \
    "s_cmp_eq_u32 s40, 0\n" \
    "s_cbranch_scc1 LO1_DEF3%=\n" \
    "buffer_store_dword v147, v61, s[48:51], 0 offen sc1\n" \
    "buffer_store_dwordx2 v[158:159], v57, s[44:47], s43 offen sc1\n" \
    "LO1_DEF3%=:\n" \
    "s_add_u32 s60, s60, 114688\n" \
    "s_add_u32 s42, s42, 8192\n" \
    "s_add_u32 s55, s52, 1\n" \
    "s_and_b32 s55, s55, 3\n" \
    "s_xor_b32 s64, s55, 2\n" \
    "v_readlane_b32 s54, v53, s55\n" \
    "v_readlane_b32 s43, v53, s64\n" \
    "s_nop 0\n" \
    "s_add_u32 s74, s54, 327680\n" \
    "LO1_NODEF%=:\n" \
    "s_cmp_ge_u32 s41, s40\n" \
    "s_cbranch_scc1 LO1_NOFG%=\n" \
    "buffer_load_dword v164, v61, s[48:51], 0 offen offset:256 sc1\n" \
    "LO1_NOFG%=:\n" \
    "s_waitcnt lgkmcnt(5)\n" \
    "v_smfmac_f32_16x16x64_bf16 v[92:95], v[68:71], a[8:15], v52\n" \
    "v_smfmac_f32_16x16x64_bf16 v[96:99], v[68:71], a[64:71], v52\n" \
    "v_smfmac_f32_16x16x64_bf16 v[100:103], v[68:71], a[120:127], v52\n" \
    "v_smfmac_f32_16x16x64_bf16 v[104:107], v[68:71], a[176:183], v52\n" \
    "s_waitcnt lgkmcnt(4)\n" \
    "v_smfmac_f32_16x16x64_bf16 v[92:95], v[72:75], a[16:23], v52\n" \
    "s_waitcnt lgkmcnt(3)\n" \
    "v_smfmac_f32_16x16x64_bf16 v[92:95], v[76:79], a[24:31], v52\n" \
    "s_waitcnt lgkmcnt(2)\n" \
    "v_smfmac_f32_16x16x64_bf16 v[92:95], v[80:83], a[32:39], v52\n" \
    "s_waitcnt lgkmcnt(1)\n" \
    "v_smfmac_f32_16x16x64_bf16 v[92:95], v[84:87], a[40:47], v52\n" \
    "s_waitcnt lgkmcnt(0)\n" \
    "v_smfmac_f32_16x16x64_bf16 v[92:95], v[88:91], a[48:55], v52\n" \
    "v_smfmac_f32_16x16x64_bf16 v[96:99], v[72:75], a[72:79], v52\n" \
    "v_smfmac_f32_16x16x64_bf16 v[96:99], v[76:79], a[80:87], v52\n" \
    "v_smfmac_f32_16x16x64_bf16 v[96:99], v[80:83], a[88:95], v52\n" \
    "v_smfmac_f32_16x16x64_bf16 v[96:99], v[84:87], a[96:103], v52\n" \
    "v_smfmac_f32_16x16x64_bf16 v[96:99], v[88:91], a[104:111], v52\n" \
    "v_smfmac_f32_16x16x64_bf16 v[100:103], v[72:75], a[128:135], v52\n" \
    "s_nop 2\n" \
    "v_smfmac_f32_16x16x64_bf16 v[100:103], v[76:79], a[136:143], v52\n" \
    "s_nop 2\n" \
    "v_smfmac_f32_16x16x64_bf16 v[100:103], v[80:83], a[144:151], v52\n" \
    "v_permlane32_swap_b32 v92, v96\n" \
    "v_permlane32_swap_b32 v93, v97\n" \
    "v_smfmac_f32_16x16x64_bf16 v[100:103], v[84:87], a[152:159], v52\n" \
    "v_permlane32_swap_b32 v94, v98\n" \
    "v_permlane32_swap_b32 v95, v99\n" \
    "v_smfmac_f32_16x16x64_bf16 v[100:103], v[88:91], a[160:167], v52\n" \
    "v_pk_add_f32 v[144:145], v[92:93], v[96:97]\n" \
    "v_pk_add_f32 v[146:147], v[94:95], v[98:99]\n" \
    "v_smfmac_f32_16x16x64_bf16 v[104:107], v[72:75], a[184:191], v52\n" \
    "v_cvt_pk_bf16_f32 v124, v144, v145\n" \
    "v_cvt_pk_bf16_f32 v125, v146, v147\n" \
    "v_smfmac_f32_16x16x64_bf16 v[104:107], v[76:79], a[192:199], v52\n" \
    "ds_write_b64 v44, v[124:125]\n" \
    "v_smfmac_f32_16x16x64_bf16 v[104:107], v[80:83], a[200:207], v52\n" \
    "v_smfmac_f32_16x16x64_bf16 v[104:107], v[84:87], a[208:215], v52\n" \
    "v_smfmac_f32_16x16x64_bf16 v[104:107], v[88:91], a[216:223], v52\n" \
    "s_nop 7\n" \
    "v_permlane32_swap_b32 v100, v104\n" \
    "v_permlane32_swap_b32 v101, v105\n" \
    "v_permlane32_swap_b32 v102, v106\n" \
    "v_permlane32_swap_b32 v103, v107\n" \
    "v_pk_add_f32 v[144:145], v[100:101], v[104:105]\n" \
    "v_pk_add_f32 v[146:147], v[102:103], v[106:107]\n" \
    "v_cvt_pk_bf16_f32 v126, v144, v145\n" \
    "v_cvt_pk_bf16_f32 v127, v146, v147\n" \
    "ds_write_b64 v44, v[126:127] offset:512\n" \
    "s_cmp_eq_u32 s52, 63\n" \
    "s_cbranch_scc1 LO1_LAST%=\n" \
    "ds_read_b128 v[128:131], v45\n" \
    "s_mov_b32 s58, s62\n" \
    "LO1_DS%=:\n" \
    "s_sub_u32 s58, s58, 1\n" \
    "s_cbranch_scc0 LO1_DS%=\n" \
    "s_waitcnt vmcnt(0)\n" \
    "buffer_load_dwordx2 v[108:109], v42, s[44:47], s53 offen offset:0 sc1\n" \
    "buffer_load_dwordx2 v[110:111], v42, s[44:47], s53 offen offset:128 sc1\n" \
    "buffer_load_dwordx2 v[112:113], v42, s[44:47], s53 offen offset:256 sc1\n" \
    "buffer_load_dwordx2 v[114:115], v42, s[44:47], s53 offen offset:384 sc1\n" \
    "buffer_load_dword v120, v48, s[44:47], s53 offen sc1\n" \
    "buffer_load_dwordx2 v[116:117], v41, s[44:47], s53 offen sc1\n" \
    "buffer_load_dwordx2 v[118:119], v41, s[44:47], s53 offen offset:128 sc1\n" \
    "s_sleep 2\n" \
    "buffer_load_dwordx2 v[168:169], v42, s[44:47], s53 offen offset:0 sc1\n" \
    "buffer_load_dwordx2 v[170:171], v42, s[44:47], s53 offen offset:128 sc1\n" \
    "buffer_load_dwordx2 v[172:173], v42, s[44:47], s53 offen offset:256 sc1\n" \
    "buffer_load_dwordx2 v[174:175], v42, s[44:47], s53 offen offset:384 sc1\n" \
    "buffer_load_dword v180, v48, s[44:47], s53 offen sc1\n" \
    "buffer_load_dwordx2 v[176:177], v41, s[44:47], s53 offen sc1\n" \
    "buffer_load_dwordx2 v[178:179], v41, s[44:47], s53 offen offset:128 sc1\n" \
    "s_mov_b32 s66, 0\n" \
    "s_waitcnt lgkmcnt(0)\n" \
    "v_readfirstlane_b32 s57, v162\n" \
    "s_waitcnt vmcnt(9)\n" \
    "LO1_MP%=:\n" \
    "v_pk_add_f32 v[144:145], v[108:109], v[110:111]\n" \
    "v_pk_add_f32 v[146:147], v[112:113], v[114:115]\n" \
    "v_min_f32 v155, v120, v49\n" \
    "v_pk_add_f32 v[144:145], v[144:145], v[146:147]\n" \
    "v_max3_f32 v148, v144, v145, v155\n" \
    "v_max3_u32 v160, v108, v109, v110\n" \
    "v_max3_u32 v160, v160, v111, v112\n" \
    "v_max_f32_dpp v148, v148, v148 quad_perm:[1,0,3,2] row_mask:0xf bank_mask:0xf\n" \
    "v_max3_u32 v160, v160, v113, v114\n" \
    "v_max3_u32 v160, v160, v115, v120\n" \
    "v_max_f32_dpp v148, v148, v148 quad_perm:[2,3,0,1] row_mask:0xf bank_mask:0xf\n" \
    "v_sub_f32 v149, v155, v148\n" \
    "v_pk_add_f32 v[150:151], v[144:145], v[148:149] op_sel_hi:[1,0] neg_lo:[0,1] neg_hi:[0,1]\n" \
    "v_exp_f32 v149, v149\n" \
    "v_exp_f32 v150, v150\n" \
    "v_exp_f32 v151, v151\n" \
    "v_mul_f32_dpp v152, v120, v149 quad_perm:[2,3,2,3] row_mask:0xf bank_mask:0xf\n" \
    "v_add_f32 v146, v150, v151\n" \
    "v_add_f32 v152, v152, v146\n" \
    "s_waitcnt vmcnt(7)\n" \
    "v_max3_u32 v160, v160, v116, v117\n" \
    "v_add_f32_dpp v152, v152, v152 quad_perm:[1,0,3,2] row_mask:0xf bank_mask:0xf\n" \
    "v_max3_u32 v160, v160, v118, v119\n" \
    "v_cmp_eq_u32 vcc, -1, v160\n" \
    "v_add_f32_dpp v152, v152, v152 quad_perm:[2,3,0,1] row_mask:0xf bank_mask:0xf\n" \
    "v_rcp_f32 v152, v152\n" \
    "s_cbranch_vccnz LO1_SLOWS%=\n" \
    "LO1_CONTS%=:\n" \
    "v_pk_mul_f32 v[150:151], v[150:151], v[152:153] op_sel_hi:[1,0]\n" \
    "v_mul_f32 v149, v149, v152\n" \
    "v_cvt_pk_bf16_f32 v153, v150, v151\n" \
    "v_cvt_pk_bf16_f32 v154, v149, v149\n" \
    "s_memrealtime s[72:73]\n" \
    "v_mov_b32_dpp v136, v153 quad_perm:[0,0,0,0] row_mask:0xf bank_mask:0xf\n" \
    "v_mov_b32_dpp v137, v153 quad_perm:[1,1,1,1] row_mask:0xf bank_mask:0xf\n" \
    "v_mov_b32_dpp v138, v153 quad_perm:[2,2,2,2] row_mask:0xf bank_mask:0xf\n" \
    "v_mov_b32_dpp v139, v153 quad_perm:[3,3,3,3] row_mask:0xf bank_mask:0xf\n" \
    "v_and_b32_dpp v140, v154, v50 quad_perm:[0,0,0,0] row_mask:0xf bank_mask:0xf\n" \
    "v_and_b32_dpp v141, v154, v51 quad_perm:[0,0,0,0] row_mask:0xf bank_mask:0xf\n" \
    "v_mfma_f32_16x16x32_bf16 v[132:135], v[136:139], v[128:131], 0\n" \
    "v_and_b32_dpp v142, v154, v50 quad_perm:[1,1,1,1] row_mask:0xf bank_mask:0xf\n" \
    "v_and_b32_dpp v143, v154, v51 quad_perm:[1,1,1,1] row_mask:0xf bank_mask:0xf\n" \
    "s_nop 1\n" \
    "v_mfma_f32_16x16x32_bf16 v[132:135], v[140:143], v[116:119], v[132:135]\n" \
    "s_cmp_eq_u32 %17, 0\n" \
    "s_nop 6\n" \
    "ds_write_b32 v46, v132\n" \
    "s_waitcnt lgkmcnt(0)\n" \
    "s_barrier\n" \
    "s_cbranch_scc1 LO1_NOFINS%=\n" \
    "ds_read_b64 v[144:145], v47 offset:0\n" \
    "ds_read_b64 v[146:147], v47 offset:256\n" \
    "ds_read_b64 v[148:149], v47 offset:512\n" \
    "ds_read_b64 v[150:151], v47 offset:768\n" \
    "s_waitcnt lgkmcnt(2)\n" \
    "v_pk_add_f32 v[144:145], v[144:145], v[146:147]\n" \
    "s_waitcnt lgkmcnt(0)\n" \
    "v_pk_add_f32 v[148:149], v[148:149], v[150:151]\n" \
    "v_pk_add_f32 v[144:145], v[144:145], v[148:149]\n" \
    "v_cvt_pk_bf16_f32 v157, v144, v145\n" \
    "v_min_u32 v157, -2, v157\n" \
    "buffer_store_dword v157, v43, s[44:47], s54 offen sc1\n" \
    "buffer_store_dword v157, v43, s[44:47], s74 offen sc1\n" \
    "LO1_NOFIN%=:\n" \
    "s_mov_b32 s63, 0\n" \
    "s_mov_b32 s68, s57\n" \
    "s_add_u32 s52, s52, 1\n" \
    "s_mov_b32 s53, s54\n" \
    "s_branch LO1_LOOP%=\n" \
    "LO1_SLOW%=:\n" \
    "s_add_u32 s65, s65, 1\n" \
    "s_cmp_gt_u32 s65, 65536\n" \
    "s_cbranch_scc1 LO1_CONT%=\n" \
    "s_mov_b32 s63, 1\n" \
    "s_nop 7\n" \
    "s_branch LO1_RETRY%=\n" \
    "LO1_SLOWS%=:\n" \
    "s_cmp_lg_u32 s66, 0\n" \
    "s_cbranch_scc1 LO1_SLOWS2%=\n" \
    "s_mov_b32 s66, 1\n" \
    "s_waitcnt vmcnt(0)\n" \
    "v_mov_b64 v[108:109], v[168:169]\n" \
    "v_mov_b64 v[110:111], v[170:171]\n" \
    "v_mov_b64 v[112:113], v[172:173]\n" \
    "v_mov_b64 v[114:115], v[174:175]\n" \
    "v_mov_b64 v[116:117], v[176:177]\n" \
    "v_mov_b64 v[118:119], v[178:179]\n" \
    "v_mov_b64 v[120:121], v[180:181]\n" \
    "s_branch LO1_MP%=\n" \
    "LO1_SLOWS2%=:\n" \
    "s_mov_b32 s63, 1\n" \
    "s_add_u32 s67, s67, 1\n" \
    "s_cmp_gt_u32 s67, 65536\n" \
    "s_cbranch_scc1 LO1_CONTS%=\n" \
    "buffer_load_dwordx2 v[108:109], v42, s[44:47], s53 offen offset:0 sc1\n" \
    "buffer_load_dwordx2 v[110:111], v42, s[44:47], s53 offen offset:128 sc1\n" \
    "buffer_load_dwordx2 v[112:113], v42, s[44:47], s53 offen offset:256 sc1\n" \
    "buffer_load_dwordx2 v[114:115], v42, s[44:47], s53 offen offset:384 sc1\n" \
    "buffer_load_dword v120, v48, s[44:47], s53 offen sc1\n" \
    "buffer_load_dwordx2 v[116:117], v41, s[44:47], s53 offen sc1\n" \
    "buffer_load_dwordx2 v[118:119], v41, s[44:47], s53 offen offset:128 sc1\n" \
    "s_waitcnt vmcnt(0)\n" \
    "s_branch LO1_MP%=\n" \
    "LO1_NOFINS%=:\n" \
    "s_cmp_eq_u32 s40, 0\n" \
    "s_cbranch_scc1 LO1_NOANN%=\n" \
    "s_add_u32 s55, s57, s69\n" \
    "s_add_u32 s55, s55, s71\n" \
    "s_and_b32 s64, s57, 3\n" \
    "s_add_u32 s64, s64, s71\n" \
    "s_cmp_ge_u32 s64, 3\n" \
    "s_cselect_b32 s64, 1, 0\n" \
    "s_add_u32 s55, s55, s64\n" \
    "s_sub_u32 s64, s57, s72\n" \
    "s_sub_u32 s58, s64, 38\n" \
    "s_cmp_lt_u32 s58, 14\n" \
    "s_cselect_b32 s58, -4, 0\n" \
    "s_add_u32 s69, s69, s58\n" \
    "s_max_u32 s69, s69, %10\n" \
    "s_cmp_lt_i32 s64, 8\n" \
    "s_cselect_b32 s63, 1, s63\n" \
    "s_add_u32 s64, s72, s56\n" \
    "s_andn2_b32 s64, s64, 3\n" \
    "s_cmp_lg_u32 s63, 0\n" \
    "s_cselect_b32 s55, s64, s55\n" \
    "s_cselect_b32 s64, 1, 0\n" \
    "s_cmp_gt_u32 s52, 5\n" \
    "s_cselect_b32 s64, s64, 0\n" \
    "s_add_u32 s61, s61, s64\n" \
    "s_and_b32 s64, s64, s61\n" \
    "s_lshl_b32 s64, s64, 2\n" \
    "s_add_u32 s69, s69, s64\n" \
    "v_mov_b32 v146, s55\n" \
    "v_readfirstlane_b32 s41, v164\n" \
    "s_min_u32 s41, s41, 64\n" \
    "v_mov_b32 v147, s41\n" \
    "buffer_store_dwordx2 v[146:147], v57, s[44:47], s54 offen sc1\n" \
    "s_branch LO1_NOFIN%=\n" \
    "LO1_NOANN%=:\n" \
    "s_sleep 3\n" \
    "s_branch LO1_NOFIN%=\n" \
    "LO1_LAST%=:\n" \
    "buffer_store_dwordx4 v[124:127], v55, s[48:51], s60 offen sc1\n" \
    "s_waitcnt vmcnt(0) lgkmcnt(0)\n"
#define ASM_GEMM0 \
    "s_mov_b32 s48, %1\n" \
    "s_mov_b32 s49, %2\n" \
    "s_mov_b32 s50, %3\n" \
    "s_mov_b32 s51, %4\n" \
    "s_mov_b32 s58, m0\n" \
    "v_mov_b32 v32, %0\n" \
    "v_mov_b32 v34, 0\n" \
    "v_mov_b32 v35, 1\n" \
    "v_mov_b32 v36, 0x7fffff00\n" \
    "s_mov_b32 s52, %6\n" \
    "s_mov_b32 s56, 0\n" \
    "s_mov_b32 s59, %10\n" \
    "s_mul_i32 s62, %12, 28672\n" \
    "s_mul_i32 s63, %12, 458752\n" \
    "s_sub_u32 s53, %7, s63\n" \
    "s_add_u32 s57, %8, %9\n" \
    "s_add_u32 m0, s57, 0\n" \
    "s_add_u32 s66, s52, 0\n" \
    "buffer_load_dwordx4 v32, s[48:51], s66 offen lds\n" \
    "buffer_load_dwordx4 v32, s[48:51], s66 offen offset:1024 lds\n" \
    "buffer_load_dwordx4 v32, s[48:51], s66 offen offset:2048 lds\n" \
    "buffer_load_dwordx4 v32, s[48:51], s66 offen offset:3072 lds\n" \
    "s_add_u32 m0, s57, 4096\n" \
    "s_add_u32 s66, s52, 4096\n" \
    "buffer_load_dwordx4 v32, s[48:51], s66 offen lds\n" \
    "buffer_load_dwordx4 v32, s[48:51], s66 offen offset:1024 lds\n" \
    "buffer_load_dwordx4 v32, s[48:51], s66 offen offset:2048 lds\n" \
    "s_mov_b32 s64, %5\n" \
    "buffer_load_dwordx4 a[0:3], v32, s[48:51], s64 offen offset:0\n" \
    "buffer_load_dwordx4 a[4:7], v32, s[48:51], s64 offen offset:1024\n" \
    "buffer_load_dwordx4 a[8:11], v32, s[48:51], s64 offen offset:2048\n" \
    "buffer_load_dwordx4 a[12:15], v32, s[48:51], s64 offen offset:3072\n" \
    "s_add_u32 s64, s64, 4096\n" \
    "buffer_load_dwordx4 a[16:19], v32, s[48:51], s64 offen offset:0\n" \
    "buffer_load_dwordx4 a[20:23], v32, s[48:51], s64 offen offset:1024\n" \
    "buffer_load_dwordx4 a[24:27], v32, s[48:51], s64 offen offset:2048\n" \
    "buffer_load_dwordx4 a[28:31], v32, s[48:51], s64 offen offset:3072\n" \
    "s_add_u32 s64, s64, 4096\n" \
    "buffer_load_dwordx4 a[32:35], v32, s[48:51], s64 offen offset:0\n" \
    "buffer_load_dwordx4 a[36:39], v32, s[48:51], s64 offen offset:1024\n" \
    "buffer_load_dwordx4 a[40:43], v32, s[48:51], s64 offen offset:2048\n" \
    "buffer_load_dwordx4 a[44:47], v32, s[48:51], s64 offen offset:3072\n" \
    "s_add_u32 s64, s64, 4096\n" \
    "buffer_load_dwordx4 a[48:51], v32, s[48:51], s64 offen offset:0\n" \
    "buffer_load_dwordx4 a[52:55], v32, s[48:51], s64 offen offset:1024\n" \
    "s_add_u32 s64, s64, 2048\n" \
    "buffer_load_dwordx4 a[56:59], v32, s[48:51], s64 offen offset:0\n" \
    "buffer_load_dwordx4 a[60:63], v32, s[48:51], s64 offen offset:1024\n" \
    "buffer_load_dwordx4 a[64:67], v32, s[48:51], s64 offen offset:2048\n" \
    "buffer_load_dwordx4 a[68:71], v32, s[48:51], s64 offen offset:3072\n" \
    "s_add_u32 s64, s64, 4096\n" \
    "buffer_load_dwordx4 a[72:75], v32, s[48:51], s64 offen offset:0\n" \
    "buffer_load_dwordx4 a[76:79], v32, s[48:51], s64 offen offset:1024\n" \
    "buffer_load_dwordx4 a[80:83], v32, s[48:51], s64 offen offset:2048\n" \
    "buffer_load_dwordx4 a[84:87], v32, s[48:51], s64 offen offset:3072\n" \
    "s_add_u32 s64, s64, 4096\n" \
    "buffer_load_dwordx4 a[88:91], v32, s[48:51], s64 offen offset:0\n" \
    "buffer_load_dwordx4 a[92:95], v32, s[48:51], s64 offen offset:1024\n" \
    "buffer_load_dwordx4 a[96:99], v32, s[48:51], s64 offen offset:2048\n" \
    "buffer_load_dwordx4 a[100:103], v32, s[48:51], s64 offen offset:3072\n" \
    "s_add_u32 s64, s64, 4096\n" \
    "buffer_load_dwordx4 a[104:107], v32, s[48:51], s64 offen offset:0\n" \
    "buffer_load_dwordx4 a[108:111], v32, s[48:51], s64 offen offset:1024\n" \
    "s_add_u32 s64, s64, 2048\n" \
    "s_waitcnt vmcnt(16)\n" \
    "buffer_load_dwordx4 a[112:115], v32, s[48:51], s64 offen offset:0\n" \
    "buffer_load_dwordx4 a[116:119], v32, s[48:51], s64 offen offset:1024\n" \
    "buffer_load_dwordx4 a[120:123], v32, s[48:51], s64 offen offset:2048\n" \
    "buffer_load_dwordx4 a[124:127], v32, s[48:51], s64 offen offset:3072\n" \
    "s_add_u32 s64, s64, 4096\n" \
    "buffer_load_dwordx4 a[128:131], v32, s[48:51], s64 offen offset:0\n" \
    "buffer_load_dwordx4 a[132:135], v32, s[48:51], s64 offen offset:1024\n" \
    "buffer_load_dwordx4 a[136:139], v32, s[48:51], s64 offen offset:2048\n" \
    "buffer_load_dwordx4 a[140:143], v32, s[48:51], s64 offen offset:3072\n" \
    "s_add_u32 s64, s64, 4096\n" \
    "buffer_load_dwordx4 a[144:147], v32, s[48:51], s64 offen offset:0\n" \
    "buffer_load_dwordx4 a[148:151], v32, s[48:51], s64 offen offset:1024\n" \
    "buffer_load_dwordx4 a[152:155], v32, s[48:51], s64 offen offset:2048\n" \
    "buffer_load_dwordx4 a[156:159], v32, s[48:51], s64 offen offset:3072\n" \
    "s_add_u32 s64, s64, 4096\n" \
    "buffer_load_dwordx4 a[160:163], v32, s[48:51], s64 offen offset:0\n" \
    "buffer_load_dwordx4 a[164:167], v32, s[48:51], s64 offen offset:1024\n" \
    "s_add_u32 s64, s64, 2048\n" \
    "buffer_load_dwordx4 a[168:171], v32, s[48:51], s64 offen offset:0\n" \
    "buffer_load_dwordx4 a[172:175], v32, s[48:51], s64 offen offset:1024\n" \
    "buffer_load_dwordx4 a[176:179], v32, s[48:51], s64 offen offset:2048\n" \
    "buffer_load_dwordx4 a[180:183], v32, s[48:51], s64 offen offset:3072\n" \
    "s_add_u32 s64, s64, 4096\n" \
    "buffer_load_dwordx4 a[184:187], v32, s[48:51], s64 offen offset:0\n" \
    "buffer_load_dwordx4 a[188:191], v32, s[48:51], s64 offen offset:1024\n" \
    "buffer_load_dwordx4 a[192:195], v32, s[48:51], s64 offen offset:2048\n" \
    "buffer_load_dwordx4 a[196:199], v32, s[48:51], s64 offen offset:3072\n" \
    "s_add_u32 s64, s64, 4096\n" \
    "buffer_load_dwordx4 a[200:203], v32, s[48:51], s64 offen offset:0\n" \
    "buffer_load_dwordx4 a[204:207], v32, s[48:51], s64 offen offset:1024\n" \
    "buffer_load_dwordx4 a[208:211], v32, s[48:51], s64 offen offset:2048\n" \
    "buffer_load_dwordx4 a[212:215], v32, s[48:51], s64 offen offset:3072\n" \
    "s_add_u32 s64, s64, 4096\n" \
    "buffer_load_dwordx4 a[216:219], v32, s[48:51], s64 offen offset:0\n" \
    "buffer_load_dwordx4 a[220:223], v32, s[48:51], s64 offen offset:1024\n" \
    "s_add_u32 s64, s64, 2048\n" \
    "v_add_u32 v33, %8, v32\n" \
    "s_waitcnt vmcnt(0)\n" \
    "LG0_LOOP%=:\n" \
    "s_barrier\n" \
    "ds_read_b128 v[40:43], v33 offset:0\n" \
    "ds_read_b128 v[44:47], v33 offset:14336\n" \
    "ds_read_b128 v[48:51], v33 offset:1024\n" \
    "ds_read_b128 v[52:55], v33 offset:15360\n" \
    "s_add_u32 s55, s56, 1\n" \
    "s_cmp_lt_u32 s55, %13\n" \
    "s_cselect_b32 s55, s62, 0\n" \
    "s_add_u32 s52, s52, s55\n" \
    "s_xor_b32 s57, s57, 0x8000\n" \
    "s_add_u32 s54, s53, 14336\n" \
    "s_cmp_lt_u32 s56, 6\n" \
    "s_cselect_b32 s55, 0, %11\n" \
    "s_cmp_eq_u32 s55, 0\n" \
    "s_cbranch_scc1 LG0_NS0%=\n" \
    "s_sub_u32 s55, s56, 2\n" \
    "s_mul_i32 s55, s55, %12\n" \
    "s_lshl_b32 s55, s55, 2\n" \
    "s_add_u32 s55, s55, s59\n" \
    "s_mov_b64 s[60:61], exec\n" \
    "s_mov_b64 exec, 1\n" \
    "buffer_atomic_add v35, v34, s[48:51], s55 offen\n" \
    "s_mov_b64 exec, s[60:61]\n" \
    "LG0_NS0%=:\n" \
    "s_waitcnt lgkmcnt(3)\n" \
    "v_mfma_f32_16x16x32_bf16 v[64:67], a[0:3], v[40:43], 0\n" \
    "ds_read_b128 v[56:59], v33 offset:2048\n" \
    "v_mfma_f32_16x16x32_bf16 v[72:75], a[56:59], v[40:43], 0\n" \
    "s_add_u32 m0, s57, 0\n" \
    "s_add_u32 s66, s52, 0\n" \
    "buffer_load_dwordx4 v32, s[48:51], s66 offen lds\n" \
    "v_mfma_f32_16x16x32_bf16 v[80:83], a[112:115], v[40:43], 0\n" \
    "v_mfma_f32_16x16x32_bf16 v[88:91], a[168:171], v[40:43], 0\n" \
    "buffer_load_dwordx4 v32, s[48:51], s66 offen offset:1024 lds\n" \
    "s_waitcnt lgkmcnt(3)\n" \
    "v_mfma_f32_16x16x32_bf16 v[68:71], a[0:3], v[44:47], 0\n" \
    "ds_read_b128 v[60:63], v33 offset:16384\n" \
    "v_mfma_f32_16x16x32_bf16 v[76:79], a[56:59], v[44:47], 0\n" \
    "buffer_load_dwordx4 v32, s[48:51], s66 offen offset:2048 lds\n" \
    "v_mfma_f32_16x16x32_bf16 v[84:87], a[112:115], v[44:47], 0\n" \
    "v_mfma_f32_16x16x32_bf16 v[92:95], a[168:171], v[44:47], 0\n" \
    "buffer_load_dwordx4 v32, s[48:51], s66 offen offset:3072 lds\n" \
    "s_waitcnt lgkmcnt(3)\n" \
    "v_mfma_f32_16x16x32_bf16 v[64:67], a[4:7], v[48:51], v[64:67]\n" \
    "ds_read_b128 v[40:43], v33 offset:3072\n" \
    "v_mfma_f32_16x16x32_bf16 v[72:75], a[60:63], v[48:51], v[72:75]\n" \
    "s_add_u32 m0, s57, 4096\n" \
    "s_add_u32 s66, s52, 4096\n" \
    "buffer_load_dwordx4 v32, s[48:51], s66 offen lds\n" \
    "v_mfma_f32_16x16x32_bf16 v[80:83], a[116:119], v[48:51], v[80:83]\n" \
    "v_mfma_f32_16x16x32_bf16 v[88:91], a[172:175], v[48:51], v[88:91]\n" \
    "buffer_load_dwordx4 v32, s[48:51], s66 offen offset:1024 lds\n" \
    "s_waitcnt lgkmcnt(3)\n" \
    "v_mfma_f32_16x16x32_bf16 v[68:71], a[4:7], v[52:55], v[68:71]\n" \
    "ds_read_b128 v[44:47], v33 offset:17408\n" \
    "v_mfma_f32_16x16x32_bf16 v[76:79], a[60:63], v[52:55], v[76:79]\n" \
    "buffer_load_dwordx4 v32, s[48:51], s66 offen offset:2048 lds\n" \
    "v_mfma_f32_16x16x32_bf16 v[84:87], a[116:119], v[52:55], v[84:87]\n" \
    "v_mfma_f32_16x16x32_bf16 v[92:95], a[172:175], v[52:55], v[92:95]\n" \
    "v_cvt_pk_bf16_f32 v128, v96, v97\n" \
    "v_cvt_pk_bf16_f32 v129, v98, v99\n" \
    "s_waitcnt lgkmcnt(3)\n" \
    "v_mfma_f32_16x16x32_bf16 v[64:67], a[8:11], v[56:59], v[64:67]\n" \
    "ds_read_b128 v[48:51], v33 offset:4096\n" \
    "v_mfma_f32_16x16x32_bf16 v[72:75], a[64:67], v[56:59], v[72:75]\n" \
    "v_cvt_pk_bf16_f32 v130, v104, v105\n" \
    "v_cvt_pk_bf16_f32 v131, v106, v107\n" \
    "v_mfma_f32_16x16x32_bf16 v[80:83], a[120:123], v[56:59], v[80:83]\n" \
    "v_mfma_f32_16x16x32_bf16 v[88:91], a[176:179], v[56:59], v[88:91]\n" \
    "buffer_store_dwordx4 v[128:131], v36, s[48:51], s53 offen offset:0 sc1\n" \
    "s_waitcnt lgkmcnt(3)\n" \
    "v_mfma_f32_16x16x32_bf16 v[68:71], a[8:11], v[60:63], v[68:71]\n" \
    "ds_read_b128 v[52:55], v33 offset:18432\n" \
    "v_mfma_f32_16x16x32_bf16 v[76:79], a[64:67], v[60:63], v[76:79]\n" \
    "v_cvt_pk_bf16_f32 v132, v112, v113\n" \
    "v_cvt_pk_bf16_f32 v133, v114, v115\n" \
    "v_mfma_f32_16x16x32_bf16 v[84:87], a[120:123], v[60:63], v[84:87]\n" \
    "v_mfma_f32_16x16x32_bf16 v[92:95], a[176:179], v[60:63], v[92:95]\n" \
    "v_cvt_pk_bf16_f32 v134, v120, v121\n" \
    "v_cvt_pk_bf16_f32 v135, v122, v123\n" \
    "s_waitcnt lgkmcnt(3)\n" \
    "v_mfma_f32_16x16x32_bf16 v[64:67], a[12:15], v[40:43], v[64:67]\n" \
    "ds_read_b128 v[56:59], v33 offset:5120\n" \
    "v_mfma_f32_16x16x32_bf16 v[72:75], a[68:71], v[40:43], v[72:75]\n" \
    "buffer_store_dwordx4 v[132:135], v36, s[48:51], s53 offen offset:1024 sc1\n" \
    "v_mfma_f32_16x16x32_bf16 v[80:83], a[124:127], v[40:43], v[80:83]\n" \
    "v_mfma_f32_16x16x32_bf16 v[88:91], a[180:183], v[40:43], v[88:91]\n" \
    "v_cvt_pk_bf16_f32 v136, v100, v101\n" \
    "v_cvt_pk_bf16_f32 v137, v102, v103\n" \
    "s_waitcnt lgkmcnt(3)\n" \
    "v_mfma_f32_16x16x32_bf16 v[68:71], a[12:15], v[44:47], v[68:71]\n" \
    "ds_read_b128 v[60:63], v33 offset:19456\n" \
    "v_mfma_f32_16x16x32_bf16 v[76:79], a[68:71], v[44:47], v[76:79]\n" \
    "v_cvt_pk_bf16_f32 v138, v108, v109\n" \
    "v_cvt_pk_bf16_f32 v139, v110, v111\n" \
    "v_mfma_f32_16x16x32_bf16 v[84:87], a[124:127], v[44:47], v[84:87]\n" \
    "v_mfma_f32_16x16x32_bf16 v[92:95], a[180:183], v[44:47], v[92:95]\n" \
    "buffer_store_dwordx4 v[136:139], v36, s[48:51], s54 offen offset:0 sc1\n" \
    "s_waitcnt lgkmcnt(3)\n" \
    "v_mfma_f32_16x16x32_bf16 v[64:67], a[16:19], v[48:51], v[64:67]\n" \
    "ds_read_b128 v[40:43], v33 offset:6144\n" \
    "v_mfma_f32_16x16x32_bf16 v[72:75], a[72:75], v[48:51], v[72:75]\n" \
    "v_cvt_pk_bf16_f32 v140, v116, v117\n" \
    "v_cvt_pk_bf16_f32 v141, v118, v119\n" \
    "v_mfma_f32_16x16x32_bf16 v[80:83], a[128:131], v[48:51], v[80:83]\n" \
    "v_mfma_f32_16x16x32_bf16 v[88:91], a[184:187], v[48:51], v[88:91]\n" \
    "v_cvt_pk_bf16_f32 v142, v124, v125\n" \
    "v_cvt_pk_bf16_f32 v143, v126, v127\n" \
    "s_waitcnt lgkmcnt(3)\n" \
    "v_mfma_f32_16x16x32_bf16 v[68:71], a[16:19], v[52:55], v[68:71]\n" \
    "ds_read_b128 v[44:47], v33 offset:20480\n" \
    "v_mfma_f32_16x16x32_bf16 v[76:79], a[72:75], v[52:55], v[76:79]\n" \
    "buffer_store_dwordx4 v[140:143], v36, s[48:51], s54 offen offset:1024 sc1\n" \
    "v_mfma_f32_16x16x32_bf16 v[84:87], a[128:131], v[52:55], v[84:87]\n" \
    "v_mfma_f32_16x16x32_bf16 v[92:95], a[184:187], v[52:55], v[92:95]\n" \
    "s_waitcnt lgkmcnt(3)\n" \
    "v_mfma_f32_16x16x32_bf16 v[64:67], a[20:23], v[56:59], v[64:67]\n" \
    "ds_read_b128 v[48:51], v33 offset:7168\n" \
    "v_mfma_f32_16x16x32_bf16 v[72:75], a[76:79], v[56:59], v[72:75]\n" \
    "v_mfma_f32_16x16x32_bf16 v[80:83], a[132:135], v[56:59], v[80:83]\n" \
    "v_mfma_f32_16x16x32_bf16 v[88:91], a[188:191], v[56:59], v[88:91]\n" \
    "s_waitcnt lgkmcnt(3)\n" \
    "v_mfma_f32_16x16x32_bf16 v[68:71], a[20:23], v[60:63], v[68:71]\n" \
    "ds_read_b128 v[52:55], v33 offset:21504\n" \
    "v_mfma_f32_16x16x32_bf16 v[76:79], a[76:79], v[60:63], v[76:79]\n" \
    "v_mfma_f32_16x16x32_bf16 v[84:87], a[132:135], v[60:63], v[84:87]\n" \
    "v_mfma_f32_16x16x32_bf16 v[92:95], a[188:191], v[60:63], v[92:95]\n" \
    "s_waitcnt lgkmcnt(3)\n" \
    "v_mfma_f32_16x16x32_bf16 v[64:67], a[24:27], v[40:43], v[64:67]\n" \
    "ds_read_b128 v[56:59], v33 offset:8192\n" \
    "v_mfma_f32_16x16x32_bf16 v[72:75], a[80:83], v[40:43], v[72:75]\n" \
    "v_mfma_f32_16x16x32_bf16 v[80:83], a[136:139], v[40:43], v[80:83]\n" \
    "v_mfma_f32_16x16x32_bf16 v[88:91], a[192:195], v[40:43], v[88:91]\n" \
    "s_waitcnt lgkmcnt(3)\n" \
    "v_mfma_f32_16x16x32_bf16 v[68:71], a[24:27], v[44:47], v[68:71]\n" \
    "ds_read_b128 v[60:63], v33 offset:22528\n" \
    "v_mfma_f32_16x16x32_bf16 v[76:79], a[80:83], v[44:47], v[76:79]\n" \
    "v_mfma_f32_16x16x32_bf16 v[84:87], a[136:139], v[44:47], v[84:87]\n" \
    "v_mfma_f32_16x16x32_bf16 v[92:95], a[192:195], v[44:47], v[92:95]\n" \
    "s_waitcnt lgkmcnt(3)\n" \
    "v_mfma_f32_16x16x32_bf16 v[64:67], a[28:31], v[48:51], v[64:67]\n" \
    "ds_read_b128 v[40:43], v33 offset:9216\n" \
    "v_mfma_f32_16x16x32_bf16 v[72:75], a[84:87], v[48:51], v[72:75]\n" \
    "v_mfma_f32_16x16x32_bf16 v[80:83], a[140:143], v[48:51], v[80:83]\n" \
    "v_mfma_f32_16x16x32_bf16 v[88:91], a[196:199], v[48:51], v[88:91]\n" \
    "s_waitcnt lgkmcnt(3)\n" \
    "v_mfma_f32_16x16x32_bf16 v[68:71], a[28:31], v[52:55], v[68:71]\n" \
    "ds_read_b128 v[44:47], v33 offset:23552\n" \
    "v_mfma_f32_16x16x32_bf16 v[76:79], a[84:87], v[52:55], v[76:79]\n" \
    "v_mfma_f32_16x16x32_bf16 v[84:87], a[140:143], v[52:55], v[84:87]\n" \
    "v_mfma_f32_16x16x32_bf16 v[92:95], a[196:199], v[52:55], v[92:95]\n" \
    "s_waitcnt lgkmcnt(3)\n" \
    "v_mfma_f32_16x16x32_bf16 v[64:67], a[32:35], v[56:59], v[64:67]\n" \
    "ds_read_b128 v[48:51], v33 offset:10240\n" \
    "v_mfma_f32_16x16x32_bf16 v[72:75], a[88:91], v[56:59], v[72:75]\n" \
    "v_mfma_f32_16x16x32_bf16 v[80:83], a[144:147], v[56:59], v[80:83]\n" \
    "v_mfma_f32_16x16x32_bf16 v[88:91], a[200:203], v[56:59], v[88:91]\n" \
    "s_waitcnt lgkmcnt(3)\n" \
    "v_mfma_f32_16x16x32_bf16 v[68:71], a[32:35], v[60:63], v[68:71]\n" \
    "ds_read_b128 v[52:55], v33 offset:24576\n" \
    "v_mfma_f32_16x16x32_bf16 v[76:79], a[88:91], v[60:63], v[76:79]\n" \
    "v_mfma_f32_16x16x32_bf16 v[84:87], a[144:147], v[60:63], v[84:87]\n" \
    "v_mfma_f32_16x16x32_bf16 v[92:95], a[200:203], v[60:63], v[92:95]\n" \
    "s_waitcnt lgkmcnt(3)\n" \
    "v_mfma_f32_16x16x32_bf16 v[64:67], a[36:39], v[40:43], v[64:67]\n" \
    "ds_read_b128 v[56:59], v33 offset:11264\n" \
    "v_mfma_f32_16x16x32_bf16 v[72:75], a[92:95], v[40:43], v[72:75]\n" \
    "v_mfma_f32_16x16x32_bf16 v[80:83], a[148:151], v[40:43], v[80:83]\n" \
    "v_mfma_f32_16x16x32_bf16 v[88:91], a[204:207], v[40:43], v[88:91]\n" \
    "s_waitcnt lgkmcnt(3)\n" \
    "v_mfma_f32_16x16x32_bf16 v[68:71], a[36:39], v[44:47], v[68:71]\n" \
    "ds_read_b128 v[60:63], v33 offset:25600\n" \
    "v_mfma_f32_16x16x32_bf16 v[76:79], a[92:95], v[44:47], v[76:79]\n" \
    "v_mfma_f32_16x16x32_bf16 v[84:87], a[148:151], v[44:47], v[84:87]\n" \
    "v_mfma_f32_16x16x32_bf16 v[92:95], a[204:207], v[44:47], v[92:95]\n" \
    "s_waitcnt lgkmcnt(3)\n" \
    "v_mfma_f32_16x16x32_bf16 v[64:67], a[40:43], v[48:51], v[64:67]\n" \
    "ds_read_b128 v[40:43], v33 offset:12288\n" \
    "v_mfma_f32_16x16x32_bf16 v[72:75], a[96:99], v[48:51], v[72:75]\n" \
    "v_mfma_f32_16x16x32_bf16 v[80:83], a[152:155], v[48:51], v[80:83]\n" \
    "v_mfma_f32_16x16x32_bf16 v[88:91], a[208:211], v[48:51], v[88:91]\n" \
    "s_waitcnt lgkmcnt(3)\n" \
    "v_mfma_f32_16x16x32_bf16 v[68:71], a[40:43], v[52:55], v[68:71]\n" \
    "ds_read_b128 v[44:47], v33 offset:26624\n" \
    "v_mfma_f32_16x16x32_bf16 v[76:79], a[96:99], v[52:55], v[76:79]\n" \
    "v_mfma_f32_16x16x32_bf16 v[84:87], a[152:155], v[52:55], v[84:87]\n" \
    "v_mfma_f32_16x16x32_bf16 v[92:95], a[208:211], v[52:55], v[92:95]\n" \
    "s_waitcnt lgkmcnt(3)\n" \
    "v_mfma_f32_16x16x32_bf16 v[64:67], a[44:47], v[56:59], v[64:67]\n" \
    "ds_read_b128 v[48:51], v33 offset:13312\n" \
    "v_mfma_f32_16x16x32_bf16 v[72:75], a[100:103], v[56:59], v[72:75]\n" \
    "v_mfma_f32_16x16x32_bf16 v[80:83], a[156:159], v[56:59], v[80:83]\n" \
    "v_mfma_f32_16x16x32_bf16 v[88:91], a[212:215], v[56:59], v[88:91]\n" \
    "s_waitcnt lgkmcnt(3)\n" \
    "v_mfma_f32_16x16x32_bf16 v[68:71], a[44:47], v[60:63], v[68:71]\n" \
    "ds_read_b128 v[52:55], v33 offset:27648\n" \
    "v_mfma_f32_16x16x32_bf16 v[76:79], a[100:103], v[60:63], v[76:79]\n" \
    "v_mfma_f32_16x16x32_bf16 v[84:87], a[156:159], v[60:63], v[84:87]\n" \
    "v_mfma_f32_16x16x32_bf16 v[92:95], a[212:215], v[60:63], v[92:95]\n" \
    "s_waitcnt lgkmcnt(3)\n" \
    "v_mfma_f32_16x16x32_bf16 v[64:67], a[48:51], v[40:43], v[64:67]\n" \
    "v_mfma_f32_16x16x32_bf16 v[72:75], a[104:107], v[40:43], v[72:75]\n" \
    "v_mfma_f32_16x16x32_bf16 v[80:83], a[160:163], v[40:43], v[80:83]\n" \
    "v_mfma_f32_16x16x32_bf16 v[88:91], a[216:219], v[40:43], v[88:91]\n" \
    "s_waitcnt lgkmcnt(2)\n" \
    "v_mfma_f32_16x16x32_bf16 v[68:71], a[48:51], v[44:47], v[68:71]\n" \
    "v_mfma_f32_16x16x32_bf16 v[76:79], a[104:107], v[44:47], v[76:79]\n" \
    "v_mfma_f32_16x16x32_bf16 v[84:87], a[160:163], v[44:47], v[84:87]\n" \
    "v_mfma_f32_16x16x32_bf16 v[92:95], a[216:219], v[44:47], v[92:95]\n" \
    "s_waitcnt vmcnt(0)\n" \
    "s_waitcnt lgkmcnt(1)\n" \
    "v_mfma_f32_16x16x32_bf16 v[64:67], a[52:55], v[48:51], v[64:67]\n" \
    "v_mfma_f32_16x16x32_bf16 v[72:75], a[108:111], v[48:51], v[72:75]\n" \
    "v_mfma_f32_16x16x32_bf16 v[80:83], a[164:167], v[48:51], v[80:83]\n" \
    "v_mfma_f32_16x16x32_bf16 v[88:91], a[220:223], v[48:51], v[88:91]\n" \
    "s_waitcnt lgkmcnt(0)\n" \
    "v_mfma_f32_16x16x32_bf16 v[68:71], a[52:55], v[52:55], v[68:71]\n" \
    "v_mfma_f32_16x16x32_bf16 v[76:79], a[108:111], v[52:55], v[76:79]\n" \
    "v_mfma_f32_16x16x32_bf16 v[84:87], a[164:167], v[52:55], v[84:87]\n" \
    "v_mfma_f32_16x16x32_bf16 v[92:95], a[220:223], v[52:55], v[92:95]\n" \
    "v_xor_b32 v33, 0x8000, v33\n" \
    "v_mov_b32 v36, v32\n" \
    "s_add_u32 s53, s53, s63\n" \
    "s_cmp_gt_u32 s56, 3\n" \
    "s_cbranch_scc1 LG0_NE0%=\n" \
    "s_add_u32 s54, s53, 14336\n" \
    "s_nop 3\n" \
    "v_cvt_pk_bf16_f32 v128, v64, v65\n" \
    "v_cvt_pk_bf16_f32 v129, v66, v67\n" \
    "v_cvt_pk_bf16_f32 v130, v72, v73\n" \
    "v_cvt_pk_bf16_f32 v131, v74, v75\n" \
    "buffer_store_dwordx4 v[128:131], v36, s[48:51], s53 offen offset:0 sc1\n" \
    "v_cvt_pk_bf16_f32 v132, v80, v81\n" \
    "v_cvt_pk_bf16_f32 v133, v82, v83\n" \
    "v_cvt_pk_bf16_f32 v134, v88, v89\n" \
    "v_cvt_pk_bf16_f32 v135, v90, v91\n" \
    "buffer_store_dwordx4 v[132:135], v36, s[48:51], s53 offen offset:1024 sc1\n" \
    "v_cvt_pk_bf16_f32 v136, v68, v69\n" \
    "v_cvt_pk_bf16_f32 v137, v70, v71\n" \
    "v_cvt_pk_bf16_f32 v138, v76, v77\n" \
    "v_cvt_pk_bf16_f32 v139, v78, v79\n" \
    "buffer_store_dwordx4 v[136:139], v36, s[48:51], s54 offen offset:0 sc1\n" \
    "v_cvt_pk_bf16_f32 v140, v84, v85\n" \
    "v_cvt_pk_bf16_f32 v141, v86, v87\n" \
    "v_cvt_pk_bf16_f32 v142, v92, v93\n" \
    "v_cvt_pk_bf16_f32 v143, v94, v95\n" \
    "buffer_store_dwordx4 v[140:143], v36, s[48:51], s54 offen offset:1024 sc1\n" \
    "s_waitcnt vmcnt(0)\n" \
    "s_barrier\n" \
    "s_cmp_eq_u32 %11, 0\n" \
    "s_cbranch_scc1 LG0_NE0%=\n" \
    "s_sub_u32 s55, s56, 0\n" \
    "s_mul_i32 s55, s55, %12\n" \
    "s_lshl_b32 s55, s55, 2\n" \
    "s_add_u32 s55, s55, s59\n" \
    "s_mov_b64 s[60:61], exec\n" \
    "s_mov_b64 exec, 1\n" \
    "buffer_atomic_add v35, v34, s[48:51], s55 offen\n" \
    "s_mov_b64 exec, s[60:61]\n" \
    "LG0_NE0%=:\n" \
    "s_add_u32 s56, s56, 1\n" \
    "s_barrier\n" \
    "ds_read_b128 v[40:43], v33 offset:0\n" \
    "ds_read_b128 v[44:47], v33 offset:14336\n" \
    "ds_read_b128 v[48:51], v33 offset:1024\n" \
    "ds_read_b128 v[52:55], v33 offset:15360\n" \
    "s_add_u32 s55, s56, 1\n" \
    "s_cmp_lt_u32 s55, %13\n" \
    "s_cselect_b32 s55, s62, 0\n" \
    "s_add_u32 s52, s52, s55\n" \
    "s_xor_b32 s57, s57, 0x8000\n" \
    "s_add_u32 s54, s53, 14336\n" \
    "s_cmp_lt_u32 s56, 6\n" \
    "s_cselect_b32 s55, 0, %11\n" \
    "s_cmp_eq_u32 s55, 0\n" \
    "s_cbranch_scc1 LG0_NS1%=\n" \
    "s_sub_u32 s55, s56, 2\n" \
    "s_mul_i32 s55, s55, %12\n" \
    "s_lshl_b32 s55, s55, 2\n" \
    "s_add_u32 s55, s55, s59\n" \
    "s_mov_b64 s[60:61], exec\n" \
    "s_mov_b64 exec, 1\n" \
    "buffer_atomic_add v35, v34, s[48:51], s55 offen\n" \
    "s_mov_b64 exec, s[60:61]\n" \
    "LG0_NS1%=:\n" \
    "s_waitcnt lgkmcnt(3)\n" \
    "v_mfma_f32_16x16x32_bf16 v[96:99], a[0:3], v[40:43], 0\n" \
    "ds_read_b128 v[56:59], v33 offset:2048\n" \
    "v_mfma_f32_16x16x32_bf16 v[104:107], a[56:59], v[40:43], 0\n" \
    "s_add_u32 m0, s57, 0\n" \
    "s_add_u32 s66, s52, 0\n" \
    "buffer_load_dwordx4 v32, s[48:51], s66 offen lds\n" \
    "v_mfma_f32_16x16x32_bf16 v[112:115], a[112:115], v[40:43], 0\n" \
    "v_mfma_f32_16x16x32_bf16 v[120:123], a[168:171], v[40:43], 0\n" \
    "buffer_load_dwordx4 v32, s[48:51], s66 offen offset:1024 lds\n" \
    "s_waitcnt lgkmcnt(3)\n" \
    "v_mfma_f32_16x16x32_bf16 v[100:103], a[0:3], v[44:47], 0\n" \
    "ds_read_b128 v[60:63], v33 offset:16384\n" \
    "v_mfma_f32_16x16x32_bf16 v[108:111], a[56:59], v[44:47], 0\n" \
    "buffer_load_dwordx4 v32, s[48:51], s66 offen offset:2048 lds\n" \
    "v_mfma_f32_16x16x32_bf16 v[116:119], a[112:115], v[44:47], 0\n" \
    "v_mfma_f32_16x16x32_bf16 v[124:127], a[168:171], v[44:47], 0\n" \
    "buffer_load_dwordx4 v32, s[48:51], s66 offen offset:3072 lds\n" \
    "s_waitcnt lgkmcnt(3)\n" \
    "v_mfma_f32_16x16x32_bf16 v[96:99], a[4:7], v[48:51], v[96:99]\n" \
    "ds_read_b128 v[40:43], v33 offset:3072\n" \
    "v_mfma_f32_16x16x32_bf16 v[104:107], a[60:63], v[48:51], v[104:107]\n" \
    "s_add_u32 m0, s57, 4096\n" \
    "s_add_u32 s66, s52, 4096\n" \
    "buffer_load_dwordx4 v32, s[48:51], s66 offen lds\n" \
    "v_mfma_f32_16x16x32_bf16 v[112:115], a[116:119], v[48:51], v[112:115]\n" \
    "v_mfma_f32_16x16x32_bf16 v[120:123], a[172:175], v[48:51], v[120:123]\n" \
    "buffer_load_dwordx4 v32, s[48:51], s66 offen offset:1024 lds\n" \
    "s_waitcnt lgkmcnt(3)\n" \
    "v_mfma_f32_16x16x32_bf16 v[100:103], a[4:7], v[52:55], v[100:103]\n" \
    "ds_read_b128 v[44:47], v33 offset:17408\n" \
    "v_mfma_f32_16x16x32_bf16 v[108:111], a[60:63], v[52:55], v[108:111]\n" \
    "buffer_load_dwordx4 v32, s[48:51], s66 offen offset:2048 lds\n" \
    "v_mfma_f32_16x16x32_bf16 v[116:119], a[116:119], v[52:55], v[116:119]\n" \
    "v_mfma_f32_16x16x32_bf16 v[124:127], a[172:175], v[52:55], v[124:127]\n" \
    "v_cvt_pk_bf16_f32 v128, v64, v65\n" \
    "v_cvt_pk_bf16_f32 v129, v66, v67\n" \
    "s_waitcnt lgkmcnt(3)\n" \
    "v_mfma_f32_16x16x32_bf16 v[96:99], a[8:11], v[56:59], v[96:99]\n" \
    "ds_read_b128 v[48:51], v33 offset:4096\n" \
    "v_mfma_f32_16x16x32_bf16 v[104:107], a[64:67], v[56:59], v[104:107]\n" \
    "v_cvt_pk_bf16_f32 v130, v72, v73\n" \
    "v_cvt_pk_bf16_f32 v131, v74, v75\n" \
    "v_mfma_f32_16x16x32_bf16 v[112:115], a[120:123], v[56:59], v[112:115]\n" \
    "v_mfma_f32_16x16x32_bf16 v[120:123], a[176:179], v[56:59], v[120:123]\n" \
    "buffer_store_dwordx4 v[128:131], v36, s[48:51], s53 offen offset:0 sc1\n" \
    "s_waitcnt lgkmcnt(3)\n" \
    "v_mfma_f32_16x16x32_bf16 v[100:103], a[8:11], v[60:63], v[100:103]\n" \
    "ds_read_b128 v[52:55], v33 offset:18432\n" \
    "v_mfma_f32_16x16x32_bf16 v[108:111], a[64:67], v[60:63], v[108:111]\n" \
    "v_cvt_pk_bf16_f32 v132, v80, v81\n" \
    "v_cvt_pk_bf16_f32 v133, v82, v83\n" \
    "v_mfma_f32_16x16x32_bf16 v[116:119], a[120:123], v[60:63], v[116:119]\n" \
    "v_mfma_f32_16x16x32_bf16 v[124:127], a[176:179], v[60:63], v[124:127]\n" \
    "v_cvt_pk_bf16_f32 v134, v88, v89\n" \
    "v_cvt_pk_bf16_f32 v135, v90, v91\n" \
    "s_waitcnt lgkmcnt(3)\n" \
    "v_mfma_f32_16x16x32_bf16 v[96:99], a[12:15], v[40:43], v[96:99]\n" \
    "ds_read_b128 v[56:59], v33 offset:5120\n" \
    "v_mfma_f32_16x16x32_bf16 v[104:107], a[68:71], v[40:43], v[104:107]\n" \
    "buffer_store_dwordx4 v[132:135], v36, s[48:51], s53 offen offset:1024 sc1\n" \
    "v_mfma_f32_16x16x32_bf16 v[112:115], a[124:127], v[40:43], v[112:115]\n" \
    "v_mfma_f32_16x16x32_bf16 v[120:123], a[180:183], v[40:43], v[120:123]\n" \
    "v_cvt_pk_bf16_f32 v136, v68, v69\n" \
    "v_cvt_pk_bf16_f32 v137, v70, v71\n" \
    "s_waitcnt lgkmcnt(3)\n" \
    "v_mfma_f32_16x16x32_bf16 v[100:103], a[12:15], v[44:47], v[100:103]\n" \
    "ds_read_b128 v[60:63], v33 offset:19456\n" \
    "v_mfma_f32_16x16x32_bf16 v[108:111], a[68:71], v[44:47], v[108:111]\n" \
    "v_cvt_pk_bf16_f32 v138, v76, v77\n" \
    "v_cvt_pk_bf16_f32 v139, v78, v79\n" \
    "v_mfma_f32_16x16x32_bf16 v[116:119], a[124:127], v[44:47], v[116:119]\n" \
    "v_mfma_f32_16x16x32_bf16 v[124:127], a[180:183], v[44:47], v[124:127]\n" \
    "buffer_store_dwordx4 v[136:139], v36, s[48:51], s54 offen offset:0 sc1\n" \
    "s_waitcnt lgkmcnt(3)\n" \
    "v_mfma_f32_16x16x32_bf16 v[96:99], a[16:19], v[48:51], v[96:99]\n" \
    "ds_read_b128 v[40:43], v33 offset:6144\n" \
    "v_mfma_f32_16x16x32_bf16 v[104:107], a[72:75], v[48:51], v[104:107]\n" \
    "v_cvt_pk_bf16_f32 v140, v84, v85\n" \
    "v_cvt_pk_bf16_f32 v141, v86, v87\n" \
    "v_mfma_f32_16x16x32_bf16 v[112:115], a[128:131], v[48:51], v[112:115]\n" \
    "v_mfma_f32_16x16x32_bf16 v[120:123], a[184:187], v[48:51], v[120:123]\n" \
    "v_cvt_pk_bf16_f32 v142, v92, v93\n" \
    "v_cvt_pk_bf16_f32 v143, v94, v95\n" \
    "s_waitcnt lgkmcnt(3)\n" \
    "v_mfma_f32_16x16x32_bf16 v[100:103], a[16:19], v[52:55], v[100:103]\n" \
    "ds_read_b128 v[44:47], v33 offset:20480\n" \
    "v_mfma_f32_16x16x32_bf16 v[108:111], a[72:75], v[52:55], v[108:111]\n" \
    "buffer_store_dwordx4 v[140:143], v36, s[48:51], s54 offen offset:1024 sc1\n" \
    "v_mfma_f32_16x16x32_bf16 v[116:119], a[128:131], v[52:55], v[116:119]\n" \
    "v_mfma_f32_16x16x32_bf16 v[124:127], a[184:187], v[52:55], v[124:127]\n" \
    "s_waitcnt lgkmcnt(3)\n" \
    "v_mfma_f32_16x16x32_bf16 v[96:99], a[20:23], v[56:59], v[96:99]\n" \
    "ds_read_b128 v[48:51], v33 offset:7168\n" \
    "v_mfma_f32_16x16x32_bf16 v[104:107], a[76:79], v[56:59], v[104:107]\n" \
    "v_mfma_f32_16x16x32_bf16 v[112:115], a[132:135], v[56:59], v[112:115]\n" \
    "v_mfma_f32_16x16x32_bf16 v[120:123], a[188:191], v[56:59], v[120:123]\n" \
    "s_waitcnt lgkmcnt(3)\n" \
    "v_mfma_f32_16x16x32_bf16 v[100:103], a[20:23], v[60:63], v[100:103]\n" \
    "ds_read_b128 v[52:55], v33 offset:21504\n" \
    "v_mfma_f32_16x16x32_bf16 v[108:111], a[76:79], v[60:63], v[108:111]\n" \
    "v_mfma_f32_16x16x32_bf16 v[116:119], a[132:135], v[60:63], v[116:119]\n" \
    "v_mfma_f32_16x16x32_bf16 v[124:127], a[188:191], v[60:63], v[124:127]\n" \
    "s_waitcnt lgkmcnt(3)\n" \
    "v_mfma_f32_16x16x32_bf16 v[96:99], a[24:27], v[40:43], v[96:99]\n" \
    "ds_read_b128 v[56:59], v33 offset:8192\n" \
    "v_mfma_f32_16x16x32_bf16 v[104:107], a[80:83], v[40:43], v[104:107]\n" \
    "v_mfma_f32_16x16x32_bf16 v[112:115], a[136:139], v[40:43], v[112:115]\n" \
    "v_mfma_f32_16x16x32_bf16 v[120:123], a[192:195], v[40:43], v[120:123]\n" \
    "s_waitcnt lgkmcnt(3)\n" \
    "v_mfma_f32_16x16x32_bf16 v[100:103], a[24:27], v[44:47], v[100:103]\n" \
    "ds_read_b128 v[60:63], v33 offset:22528\n" \
    "v_mfma_f32_16x16x32_bf16 v[108:111], a[80:83], v[44:47], v[108:111]\n" \
    "v_mfma_f32_16x16x32_bf16 v[116:119], a[136:139], v[44:47], v[116:119]\n" \
    "v_mfma_f32_16x16x32_bf16 v[124:127], a[192:195], v[44:47], v[124:127]\n" \
    "s_waitcnt lgkmcnt(3)\n" \
    "v_mfma_f32_16x16x32_bf16 v[96:99], a[28:31], v[48:51], v[96:99]\n" \
    "ds_read_b128 v[40:43], v33 offset:9216\n" \
    "v_mfma_f32_16x16x32_bf16 v[104:107], a[84:87], v[48:51], v[104:107]\n" \
    "v_mfma_f32_16x16x32_bf16 v[112:115], a[140:143], v[48:51], v[112:115]\n" \
    "v_mfma_f32_16x16x32_bf16 v[120:123], a[196:199], v[48:51], v[120:123]\n" \
    "s_waitcnt lgkmcnt(3)\n" \
    "v_mfma_f32_16x16x32_bf16 v[100:103], a[28:31], v[52:55], v[100:103]\n" \
    "ds_read_b128 v[44:47], v33 offset:23552\n" \
    "v_mfma_f32_16x16x32_bf16 v[108:111], a[84:87], v[52:55], v[108:111]\n" \
    "v_mfma_f32_16x16x32_bf16 v[116:119], a[140:143], v[52:55], v[116:119]\n" \
    "v_mfma_f32_16x16x32_bf16 v[124:127], a[196:199], v[52:55], v[124:127]\n" \
    "s_waitcnt lgkmcnt(3)\n" \
    "v_mfma_f32_16x16x32_bf16 v[96:99], a[32:35], v[56:59], v[96:99]\n" \
    "ds_read_b128 v[48:51], v33 offset:10240\n" \
    "v_mfma_f32_16x16x32_bf16 v[104:107], a[88:91], v[56:59], v[104:107]\n" \
    "v_mfma_f32_16x16x32_bf16 v[112:115], a[144:147], v[56:59], v[112:115]\n" \
    "v_mfma_f32_16x16x32_bf16 v[120:123], a[200:203], v[56:59], v[120:123]\n" \
    "s_waitcnt lgkmcnt(3)\n" \
    "v_mfma_f32_16x16x32_bf16 v[100:103], a[32:35], v[60:63], v[100:103]\n" \
    "ds_read_b128 v[52:55], v33 offset:24576\n" \
    "v_mfma_f32_16x16x32_bf16 v[108:111], a[88:91], v[60:63], v[108:111]\n" \
    "v_mfma_f32_16x16x32_bf16 v[116:119], a[144:147], v[60:63], v[116:119]\n" \
    "v_mfma_f32_16x16x32_bf16 v[124:127], a[200:203], v[60:63], v[124:127]\n" \
    "s_waitcnt lgkmcnt(3)\n" \
    "v_mfma_f32_16x16x32_bf16 v[96:99], a[36:39], v[40:43], v[96:99]\n" \
    "ds_read_b128 v[56:59], v33 offset:11264\n" \
    "v_mfma_f32_16x16x32_bf16 v[104:107], a[92:95], v[40:43], v[104:107]\n" \
    "v_mfma_f32_16x16x32_bf16 v[112:115], a[148:151], v[40:43], v[112:115]\n" \
    "v_mfma_f32_16x16x32_bf16 v[120:123], a[204:207], v[40:43], v[120:123]\n" \
    "s_waitcnt lgkmcnt(3)\n" \
    "v_mfma_f32_16x16x32_bf16 v[100:103], a[36:39], v[44:47], v[100:103]\n" \
    "ds_read_b128 v[60:63], v33 offset:25600\n" \
    "v_mfma_f32_16x16x32_bf16 v[108:111], a[92:95], v[44:47], v[108:111]\n" \
    "v_mfma_f32_16x16x32_bf16 v[116:119], a[148:151], v[44:47], v[116:119]\n" \
    "v_mfma_f32_16x16x32_bf16 v[124:127], a[204:207], v[44:47], v[124:127]\n" \
    "s_waitcnt lgkmcnt(3)\n" \
    "v_mfma_f32_16x16x32_bf16 v[96:99], a[40:43], v[48:51], v[96:99]\n" \
    "ds_read_b128 v[40:43], v33 offset:12288\n" \
    "v_mfma_f32_16x16x32_bf16 v[104:107], a[96:99], v[48:51], v[104:107]\n" \
    "v_mfma_f32_16x16x32_bf16 v[112:115], a[152:155], v[48:51], v[112:115]\n" \
    "v_mfma_f32_16x16x32_bf16 v[120:123], a[208:211], v[48:51], v[120:123]\n" \
    "s_waitcnt lgkmcnt(3)\n" \
    "v_mfma_f32_16x16x32_bf16 v[100:103], a[40:43], v[52:55], v[100:103]\n" \
    "ds_read_b128 v[44:47], v33 offset:26624\n" \
    "v_mfma_f32_16x16x32_bf16 v[108:111], a[96:99], v[52:55], v[108:111]\n" \
    "v_mfma_f32_16x16x32_bf16 v[116:119], a[152:155], v[52:55], v[116:119]\n" \
    "v_mfma_f32_16x16x32_bf16 v[124:127], a[208:211], v[52:55], v[124:127]\n" \
    "s_waitcnt lgkmcnt(3)\n" \
    "v_mfma_f32_16x16x32_bf16 v[96:99], a[44:47], v[56:59], v[96:99]\n" \
    "ds_read_b128 v[48:51], v33 offset:13312\n" \
    "v_mfma_f32_16x16x32_bf16 v[104:107], a[100:103], v[56:59], v[104:107]\n" \
    "v_mfma_f32_16x16x32_bf16 v[112:115], a[156:159], v[56:59], v[112:115]\n" \
    "v_mfma_f32_16x16x32_bf16 v[120:123], a[212:215], v[56:59], v[120:123]\n" \
    "s_waitcnt lgkmcnt(3)\n" \
    "v_mfma_f32_16x16x32_bf16 v[100:103], a[44:47], v[60:63], v[100:103]\n" \
    "ds_read_b128 v[52:55], v33 offset:27648\n" \
    "v_mfma_f32_16x16x32_bf16 v[108:111], a[100:103], v[60:63], v[108:111]\n" \
    "v_mfma_f32_16x16x32_bf16 v[116:119], a[156:159], v[60:63], v[116:119]\n" \
    "v_mfma_f32_16x16x32_bf16 v[124:127], a[212:215], v[60:63], v[124:127]\n" \
    "s_waitcnt lgkmcnt(3)\n" \
    "v_mfma_f32_16x16x32_bf16 v[96:99], a[48:51], v[40:43], v[96:99]\n" \
    "v_mfma_f32_16x16x32_bf16 v[104:107], a[104:107], v[40:43], v[104:107]\n" \
    "v_mfma_f32_16x16x32_bf16 v[112:115], a[160:163], v[40:43], v[112:115]\n" \
    "v_mfma_f32_16x16x32_bf16 v[120:123], a[216:219], v[40:43], v[120:123]\n" \
    "s_waitcnt lgkmcnt(2)\n" \
    "v_mfma_f32_16x16x32_bf16 v[100:103], a[48:51], v[44:47], v[100:103]\n" \
    "v_mfma_f32_16x16x32_bf16 v[108:111], a[104:107], v[44:47], v[108:111]\n" \
    "v_mfma_f32_16x16x32_bf16 v[116:119], a[160:163], v[44:47], v[116:119]\n" \
    "v_mfma_f32_16x16x32_bf16 v[124:127], a[216:219], v[44:47], v[124:127]\n" \
    "s_waitcnt vmcnt(0)\n" \
    "s_waitcnt lgkmcnt(1)\n" \
    "v_mfma_f32_16x16x32_bf16 v[96:99], a[52:55], v[48:51], v[96:99]\n" \
    "v_mfma_f32_16x16x32_bf16 v[104:107], a[108:111], v[48:51], v[104:107]\n" \
    "v_mfma_f32_16x16x32_bf16 v[112:115], a[164:167], v[48:51], v[112:115]\n" \
    "v_mfma_f32_16x16x32_bf16 v[120:123], a[220:223], v[48:51], v[120:123]\n" \
    "s_waitcnt lgkmcnt(0)\n" \
    "v_mfma_f32_16x16x32_bf16 v[100:103], a[52:55], v[52:55], v[100:103]\n" \
    "v_mfma_f32_16x16x32_bf16 v[108:111], a[108:111], v[52:55], v[108:111]\n" \
    "v_mfma_f32_16x16x32_bf16 v[116:119], a[164:167], v[52:55], v[116:119]\n" \
    "v_mfma_f32_16x16x32_bf16 v[124:127], a[220:223], v[52:55], v[124:127]\n" \
    "v_xor_b32 v33, 0x8000, v33\n" \
    "v_mov_b32 v36, v32\n" \
    "s_add_u32 s53, s53, s63\n" \
    "s_cmp_gt_u32 s56, 3\n" \
    "s_cbranch_scc1 LG0_NE1%=\n" \
    "s_add_u32 s54, s53, 14336\n" \
    "s_nop 3\n" \
    "v_cvt_pk_bf16_f32 v128, v96, v97\n" \
    "v_cvt_pk_bf16_f32 v129, v98, v99\n" \
    "v_cvt_pk_bf16_f32 v130, v104, v105\n" \
    "v_cvt_pk_bf16_f32 v131, v106, v107\n" \
    "buffer_store_dwordx4 v[128:131], v36, s[48:51], s53 offen offset:0 sc1\n" \
    "v_cvt_pk_bf16_f32 v132, v112, v113\n" \
    "v_cvt_pk_bf16_f32 v133, v114, v115\n" \
    "v_cvt_pk_bf16_f32 v134, v120, v121\n" \
    "v_cvt_pk_bf16_f32 v135, v122, v123\n" \
    "buffer_store_dwordx4 v[132:135], v36, s[48:51], s53 offen offset:1024 sc1\n" \
    "v_cvt_pk_bf16_f32 v136, v100, v101\n" \
    "v_cvt_pk_bf16_f32 v137, v102, v103\n" \
    "v_cvt_pk_bf16_f32 v138, v108, v109\n" \
    "v_cvt_pk_bf16_f32 v139, v110, v111\n" \
    "buffer_store_dwordx4 v[136:139], v36, s[48:51], s54 offen offset:0 sc1\n" \
    "v_cvt_pk_bf16_f32 v140, v116, v117\n" \
    "v_cvt_pk_bf16_f32 v141, v118, v119\n" \
    "v_cvt_pk_bf16_f32 v142, v124, v125\n" \
    "v_cvt_pk_bf16_f32 v143, v126, v127\n" \
    "buffer_store_dwordx4 v[140:143], v36, s[48:51], s54 offen offset:1024 sc1\n" \
    "s_waitcnt vmcnt(0)\n" \
    "s_barrier\n" \
    "s_cmp_eq_u32 %11, 0\n" \
    "s_cbranch_scc1 LG0_NE1%=\n" \
    "s_sub_u32 s55, s56, 0\n" \
    "s_mul_i32 s55, s55, %12\n" \
    "s_lshl_b32 s55, s55, 2\n" \
    "s_add_u32 s55, s55, s59\n" \
    "s_mov_b64 s[60:61], exec\n" \
    "s_mov_b64 exec, 1\n" \
    "buffer_atomic_add v35, v34, s[48:51], s55 offen\n" \
    "s_mov_b64 exec, s[60:61]\n" \
    "LG0_NE1%=:\n" \
    "s_add_u32 s56, s56, 1\n" \
    "s_cmp_lt_u32 s56, %13\n" \
    "s_cbranch_scc1 LG0_LOOP%=\n" \
    "s_add_u32 s54, s53, 14336\n" \
    "s_nop 3\n" \
    "v_cvt_pk_bf16_f32 v128, v96, v97\n" \
    "v_cvt_pk_bf16_f32 v129, v98, v99\n" \
    "v_cvt_pk_bf16_f32 v130, v104, v105\n" \
    "v_cvt_pk_bf16_f32 v131, v106, v107\n" \
    "buffer_store_dwordx4 v[128:131], v36, s[48:51], s53 offen offset:0 sc1\n" \
    "v_cvt_pk_bf16_f32 v132, v112, v113\n" \
    "v_cvt_pk_bf16_f32 v133, v114, v115\n" \
    "v_cvt_pk_bf16_f32 v134, v120, v121\n" \
    "v_cvt_pk_bf16_f32 v135, v122, v123\n" \
    "buffer_store_dwordx4 v[132:135], v36, s[48:51], s53 offen offset:1024 sc1\n" \
    "v_cvt_pk_bf16_f32 v136, v100, v101\n" \
    "v_cvt_pk_bf16_f32 v137, v102, v103\n" \
    "v_cvt_pk_bf16_f32 v138, v108, v109\n" \
    "v_cvt_pk_bf16_f32 v139, v110, v111\n" \
    "buffer_store_dwordx4 v[136:139], v36, s[48:51], s54 offen offset:0 sc1\n" \
    "v_cvt_pk_bf16_f32 v140, v116, v117\n" \
    "v_cvt_pk_bf16_f32 v141, v118, v119\n" \
    "v_cvt_pk_bf16_f32 v142, v124, v125\n" \
    "v_cvt_pk_bf16_f32 v143, v126, v127\n" \
    "buffer_store_dwordx4 v[140:143], v36, s[48:51], s54 offen offset:1024 sc1\n" \
    "s_waitcnt vmcnt(0)\n" \
    "s_barrier\n" \
    "s_cmp_eq_u32 %11, 0\n" \
    "s_cbranch_scc1 LG0_END%=\n" \
    "s_sub_u32 s55, s56, 2\n" \
    "s_mul_i32 s55, s55, %12\n" \
    "s_lshl_b32 s55, s55, 2\n" \
    "s_add_u32 s55, s55, s59\n" \
    "s_mov_b64 s[60:61], exec\n" \
    "s_mov_b64 exec, 1\n" \
    "buffer_atomic_add v35, v34, s[48:51], s55 offen\n" \
    "s_mov_b64 exec, s[60:61]\n" \
    "s_sub_u32 s55, s56, 1\n" \
    "s_mul_i32 s55, s55, %12\n" \
    "s_lshl_b32 s55, s55, 2\n" \
    "s_add_u32 s55, s55, s59\n" \
    "s_mov_b64 s[60:61], exec\n" \
    "s_mov_b64 exec, 1\n" \
    "buffer_atomic_add v35, v34, s[48:51], s55 offen\n" \
    "s_mov_b64 exec, s[60:61]\n" \
    "LG0_END%=:\n" \
    "s_mov_b32 m0, s58\n" \
    "s_waitcnt vmcnt(0)\n"
#define ASM_GEMM1 \
    "s_mov_b32 s48, %1\n" \
    "s_mov_b32 s49, %2\n" \
    "s_mov_b32 s50, %3\n" \
    "s_mov_b32 s51, %4\n" \
    "s_mov_b32 s58, m0\n" \
    "v_mov_b32 v32, %0\n" \
    "v_mov_b32 v34, 0\n" \
    "v_mov_b32 v35, 1\n" \
    "v_mov_b32 v36, 0x7fffff00\n" \
    "s_mov_b32 s52, %6\n" \
    "s_mov_b32 s56, 0\n" \
    "s_mov_b32 s59, %10\n" \
    "s_mul_i32 s62, %12, 28672\n" \
    "s_mul_i32 s63, %12, 458752\n" \
    "s_sub_u32 s53, %7, s63\n" \
    "s_add_u32 s57, %8, %9\n" \
    "s_add_u32 m0, s57, 0\n" \
    "s_add_u32 s66, s52, 0\n" \
    "buffer_load_dwordx4 v32, s[48:51], s66 offen lds\n" \
    "buffer_load_dwordx4 v32, s[48:51], s66 offen offset:1024 lds\n" \
    "buffer_load_dwordx4 v32, s[48:51], s66 offen offset:2048 lds\n" \
    "buffer_load_dwordx4 v32, s[48:51], s66 offen offset:3072 lds\n" \
    "s_add_u32 m0, s57, 4096\n" \
    "s_add_u32 s66, s52, 4096\n" \
    "buffer_load_dwordx4 v32, s[48:51], s66 offen lds\n" \
    "buffer_load_dwordx4 v32, s[48:51], s66 offen offset:1024 lds\n" \
    "buffer_load_dwordx4 v32, s[48:51], s66 offen offset:2048 lds\n" \
    "s_mov_b32 s64, %5\n" \
    "buffer_load_dwordx4 a[0:3], v32, s[48:51], s64 offen offset:0\n" \
    "buffer_load_dwordx4 a[4:7], v32, s[48:51], s64 offen offset:1024\n" \
    "buffer_load_dwordx4 a[8:11], v32, s[48:51], s64 offen offset:2048\n" \
    "buffer_load_dwordx4 a[12:15], v32, s[48:51], s64 offen offset:3072\n" \
    "s_add_u32 s64, s64, 4096\n" \
    "buffer_load_dwordx4 a[16:19], v32, s[48:51], s64 offen offset:0\n" \
    "buffer_load_dwordx4 a[20:23], v32, s[48:51], s64 offen offset:1024\n" \
    "buffer_load_dwordx4 a[24:27], v32, s[48:51], s64 offen offset:2048\n" \
    "buffer_load_dwordx4 a[28:31], v32, s[48:51], s64 offen offset:3072\n" \
    "s_add_u32 s64, s64, 4096\n" \
    "buffer_load_dwordx4 a[32:35], v32, s[48:51], s64 offen offset:0\n" \
    "buffer_load_dwordx4 a[36:39], v32, s[48:51], s64 offen offset:1024\n" \
    "buffer_load_dwordx4 a[40:43], v32, s[48:51], s64 offen offset:2048\n" \
    "buffer_load_dwordx4 a[44:47], v32, s[48:51], s64 offen offset:3072\n" \
    "s_add_u32 s64, s64, 4096\n" \
    "buffer_load_dwordx4 a[48:51], v32, s[48:51], s64 offen offset:0\n" \
    "buffer_load_dwordx4 a[52:55], v32, s[48:51], s64 offen offset:1024\n" \
    "s_add_u32 s64, s64, 2048\n" \
    "buffer_load_dwordx4 a[56:59], v32, s[48:51], s64 offen offset:0\n" \
    "buffer_load_dwordx4 a[60:63], v32, s[48:51], s64 offen offset:1024\n" \
    "buffer_load_dwordx4 a[64:67], v32, s[48:51], s64 offen offset:2048\n" \
    "buffer_load_dwordx4 a[68:71], v32, s[48:51], s64 offen offset:3072\n" \
    "s_add_u32 s64, s64, 4096\n" \
    "buffer_load_dwordx4 a[72:75], v32, s[48:51], s64 offen offset:0\n" \
    "buffer_load_dwordx4 a[76:79], v32, s[48:51], s64 offen offset:1024\n" \
    "buffer_load_dwordx4 a[80:83], v32, s[48:51], s64 offen offset:2048\n" \
    "buffer_load_dwordx4 a[84:87], v32, s[48:51], s64 offen offset:3072\n" \
    "s_add_u32 s64, s64, 4096\n" \
    "buffer_load_dwordx4 a[88:91], v32, s[48:51], s64 offen offset:0\n" \
    "buffer_load_dwordx4 a[92:95], v32, s[48:51], s64 offen offset:1024\n" \
    "buffer_load_dwordx4 a[96:99], v32, s[48:51], s64 offen offset:2048\n" \
    "buffer_load_dwordx4 a[100:103], v32, s[48:51], s64 offen offset:3072\n" \
    "s_add_u32 s64, s64, 4096\n" \
    "buffer_load_dwordx4 a[104:107], v32, s[48:51], s64 offen offset:0\n" \
    "buffer_load_dwordx4 a[108:111], v32, s[48:51], s64 offen offset:1024\n" \
    "s_add_u32 s64, s64, 2048\n" \
    "s_waitcnt vmcnt(16)\n" \
    "buffer_load_dwordx4 a[112:115], v32, s[48:51], s64 offen offset:0\n" \
    "buffer_load_dwordx4 a[116:119], v32, s[48:51], s64 offen offset:1024\n" \
    "buffer_load_dwordx4 a[120:123], v32, s[48:51], s64 offen offset:2048\n" \
    "buffer_load_dwordx4 a[124:127], v32, s[48:51], s64 offen offset:3072\n" \
    "s_add_u32 s64, s64, 4096\n" \
    "buffer_load_dwordx4 a[128:131], v32, s[48:51], s64 offen offset:0\n" \
    "buffer_load_dwordx4 a[132:135], v32, s[48:51], s64 offen offset:1024\n" \
    "buffer_load_dwordx4 a[136:139], v32, s[48:51], s64 offen offset:2048\n" \
    "buffer_load_dwordx4 a[140:143], v32, s[48:51], s64 offen offset:3072\n" \
    "s_add_u32 s64, s64, 4096\n" \
    "buffer_load_dwordx4 a[144:147], v32, s[48:51], s64 offen offset:0\n" \
    "buffer_load_dwordx4 a[148:151], v32, s[48:51], s64 offen offset:1024\n" \
    "buffer_load_dwordx4 a[152:155], v32, s[48:51], s64 offen offset:2048\n" \
    "buffer_load_dwordx4 a[156:159], v32, s[48:51], s64 offen offset:3072\n" \
    "s_add_u32 s64, s64, 4096\n" \
    "buffer_load_dwordx4 a[160:163], v32, s[48:51], s64 offen offset:0\n" \
    "buffer_load_dwordx4 a[164:167], v32, s[48:51], s64 offen offset:1024\n" \
    "s_add_u32 s64, s64, 2048\n" \
    "buffer_load_dwordx4 a[168:171], v32, s[48:51], s64 offen offset:0\n" \
    "buffer_load_dwordx4 a[172:175], v32, s[48:51], s64 offen offset:1024\n" \
    "buffer_load_dwordx4 a[176:179], v32, s[48:51], s64 offen offset:2048\n" \
    "buffer_load_dwordx4 a[180:183], v32, s[48:51], s64 offen offset:3072\n" \
    "s_add_u32 s64, s64, 4096\n" \
    "buffer_load_dwordx4 a[184:187], v32, s[48:51], s64 offen offset:0\n" \
    "buffer_load_dwordx4 a[188:191], v32, s[48:51], s64 offen offset:1024\n" \
    "buffer_load_dwordx4 a[192:195], v32, s[48:51], s64 offen offset:2048\n" \
    "buffer_load_dwordx4 a[196:199], v32, s[48:51], s64 offen offset:3072\n" \
    "s_add_u32 s64, s64, 4096\n" \
    "buffer_load_dwordx4 a[200:203], v32, s[48:51], s64 offen offset:0\n" \
    "buffer_load_dwordx4 a[204:207], v32, s[48:51], s64 offen offset:1024\n" \
    "buffer_load_dwordx4 a[208:211], v32, s[48:51], s64 offen offset:2048\n" \
    "buffer_load_dwordx4 a[212:215], v32, s[48:51], s64 offen offset:3072\n" \
    "s_add_u32 s64, s64, 4096\n" \
    "buffer_load_dwordx4 a[216:219], v32, s[48:51], s64 offen offset:0\n" \
    "buffer_load_dwordx4 a[220:223], v32, s[48:51], s64 offen offset:1024\n" \
    "s_add_u32 s64, s64, 2048\n" \
    "v_add_u32 v33, %8, v32\n" \
    "s_waitcnt vmcnt(0)\n" \
    "LG1_LOOP%=:\n" \
    "s_barrier\n" \
    "ds_read_b128 v[40:43], v33 offset:0\n" \
    "ds_read_b128 v[44:47], v33 offset:14336\n" \
    "ds_read_b128 v[48:51], v33 offset:1024\n" \
    "ds_read_b128 v[52:55], v33 offset:15360\n" \
    "s_add_u32 s55, s56, 1\n" \
    "s_cmp_lt_u32 s55, %13\n" \
    "s_cselect_b32 s55, s62, 0\n" \
    "s_add_u32 s52, s52, s55\n" \
    "s_xor_b32 s57, s57, 0x8000\n" \
    "s_add_u32 s54, s53, 14336\n" \
    "s_cmp_lt_u32 s56, 6\n" \
    "s_cselect_b32 s55, 0, %11\n" \
    "s_cmp_eq_u32 s55, 0\n" \
    "s_cbranch_scc1 LG1_NS0%=\n" \
    "s_sub_u32 s55, s56, 2\n" \
    "s_mul_i32 s55, s55, %12\n" \
    "s_lshl_b32 s55, s55, 2\n" \
    "s_add_u32 s55, s55, s59\n" \
    "s_mov_b64 s[60:61], exec\n" \
    "s_mov_b64 exec, 1\n" \
    "buffer_atomic_add v35, v34, s[48:51], s55 offen\n" \
    "s_mov_b64 exec, s[60:61]\n" \
    "LG1_NS0%=:\n" \
    "s_waitcnt lgkmcnt(3)\n" \
    "v_mfma_f32_16x16x32_bf16 v[64:67], v[40:43], a[0:3], 0\n" \
    "ds_read_b128 v[56:59], v33 offset:2048\n" \
    "v_mfma_f32_16x16x32_bf16 v[72:75], v[40:43], a[56:59], 0\n" \
    "s_add_u32 m0, s57, 0\n" \
    "s_add_u32 s66, s52, 0\n" \
    "buffer_load_dwordx4 v32, s[48:51], s66 offen lds\n" \
    "v_mfma_f32_16x16x32_bf16 v[80:83], v[40:43], a[112:115], 0\n" \
    "v_mfma_f32_16x16x32_bf16 v[88:91], v[40:43], a[168:171], 0\n" \
    "buffer_load_dwordx4 v32, s[48:51], s66 offen offset:1024 lds\n" \
    "s_waitcnt lgkmcnt(3)\n" \
    "v_mfma_f32_16x16x32_bf16 v[68:71], v[44:47], a[0:3], 0\n" \
    "ds_read_b128 v[60:63], v33 offset:16384\n" \
    "v_mfma_f32_16x16x32_bf16 v[76:79], v[44:47], a[56:59], 0\n" \
    "buffer_load_dwordx4 v32, s[48:51], s66 offen offset:2048 lds\n" \
    "v_mfma_f32_16x16x32_bf16 v[84:87], v[44:47], a[112:115], 0\n" \
    "v_mfma_f32_16x16x32_bf16 v[92:95], v[44:47], a[168:171], 0\n" \
    "buffer_load_dwordx4 v32, s[48:51], s66 offen offset:3072 lds\n" \
    "s_waitcnt lgkmcnt(3)\n" \
    "v_mfma_f32_16x16x32_bf16 v[64:67], v[48:51], a[4:7], v[64:67]\n" \
    "ds_read_b128 v[40:43], v33 offset:3072\n" \
    "v_mfma_f32_16x16x32_bf16 v[72:75], v[48:51], a[60:63], v[72:75]\n" \
    "s_add_u32 m0, s57, 4096\n" \
    "s_add_u32 s66, s52, 4096\n" \
    "buffer_load_dwordx4 v32, s[48:51], s66 offen lds\n" \
    "v_mfma_f32_16x16x32_bf16 v[80:83], v[48:51], a[116:119], v[80:83]\n" \
    "v_mfma_f32_16x16x32_bf16 v[88:91], v[48:51], a[172:175], v[88:91]\n" \
    "buffer_load_dwordx4 v32, s[48:51], s66 offen offset:1024 lds\n" \
    "s_waitcnt lgkmcnt(3)\n" \
    "v_mfma_f32_16x16x32_bf16 v[68:71], v[52:55], a[4:7], v[68:71]\n" \
    "ds_read_b128 v[44:47], v33 offset:17408\n" \
    "v_mfma_f32_16x16x32_bf16 v[76:79], v[52:55], a[60:63], v[76:79]\n" \
    "buffer_load_dwordx4 v32, s[48:51], s66 offen offset:2048 lds\n" \
    "v_mfma_f32_16x16x32_bf16 v[84:87], v[52:55], a[116:119], v[84:87]\n" \
    "v_mfma_f32_16x16x32_bf16 v[92:95], v[52:55], a[172:175], v[92:95]\n" \
    "v_cvt_pk_bf16_f32 v128, v96, v97\n" \
    "v_cvt_pk_bf16_f32 v129, v98, v99\n" \
    "s_waitcnt lgkmcnt(3)\n" \
    "v_mfma_f32_16x16x32_bf16 v[64:67], v[56:59], a[8:11], v[64:67]\n" \
    "ds_read_b128 v[48:51], v33 offset:4096\n" \
    "v_mfma_f32_16x16x32_bf16 v[72:75], v[56:59], a[64:67], v[72:75]\n" \
    "v_cvt_pk_bf16_f32 v130, v100, v101\n" \
    "v_cvt_pk_bf16_f32 v131, v102, v103\n" \
    "v_mfma_f32_16x16x32_bf16 v[80:83], v[56:59], a[120:123], v[80:83]\n" \
    "v_mfma_f32_16x16x32_bf16 v[88:91], v[56:59], a[176:179], v[88:91]\n" \
    "buffer_store_dwordx4 v[128:131], v36, s[48:51], s53 offen offset:0 sc1\n" \
    "s_waitcnt lgkmcnt(3)\n" \
    "v_mfma_f32_16x16x32_bf16 v[68:71], v[60:63], a[8:11], v[68:71]\n" \
    "ds_read_b128 v[52:55], v33 offset:18432\n" \
    "v_mfma_f32_16x16x32_bf16 v[76:79], v[60:63], a[64:67], v[76:79]\n" \
    "v_cvt_pk_bf16_f32 v132, v104, v105\n" \
    "v_cvt_pk_bf16_f32 v133, v106, v107\n" \
    "v_mfma_f32_16x16x32_bf16 v[84:87], v[60:63], a[120:123], v[84:87]\n" \
    "v_mfma_f32_16x16x32_bf16 v[92:95], v[60:63], a[176:179], v[92:95]\n" \
    "v_cvt_pk_bf16_f32 v134, v108, v109\n" \
    "v_cvt_pk_bf16_f32 v135, v110, v111\n" \
    "s_waitcnt lgkmcnt(3)\n" \
    "v_mfma_f32_16x16x32_bf16 v[64:67], v[40:43], a[12:15], v[64:67]\n" \
    "ds_read_b128 v[56:59], v33 offset:5120\n" \
    "v_mfma_f32_16x16x32_bf16 v[72:75], v[40:43], a[68:71], v[72:75]\n" \
    "buffer_store_dwordx4 v[132:135], v36, s[48:51], s53 offen offset:1024 sc1\n" \
    "v_mfma_f32_16x16x32_bf16 v[80:83], v[40:43], a[124:127], v[80:83]\n" \
    "v_mfma_f32_16x16x32_bf16 v[88:91], v[40:43], a[180:183], v[88:91]\n" \
    "v_cvt_pk_bf16_f32 v136, v112, v113\n" \
    "v_cvt_pk_bf16_f32 v137, v114, v115\n" \
    "s_waitcnt lgkmcnt(3)\n" \
    "v_mfma_f32_16x16x32_bf16 v[68:71], v[44:47], a[12:15], v[68:71]\n" \
    "ds_read_b128 v[60:63], v33 offset:19456\n" \
    "v_mfma_f32_16x16x32_bf16 v[76:79], v[44:47], a[68:71], v[76:79]\n" \
    "v_cvt_pk_bf16_f32 v138, v116, v117\n" \
    "v_cvt_pk_bf16_f32 v139, v118, v119\n" \
    "v_mfma_f32_16x16x32_bf16 v[84:87], v[44:47], a[124:127], v[84:87]\n" \
    "v_mfma_f32_16x16x32_bf16 v[92:95], v[44:47], a[180:183], v[92:95]\n" \
    "buffer_store_dwordx4 v[136:139], v36, s[48:51], s53 offen offset:2048 sc1\n" \
    "s_waitcnt lgkmcnt(3)\n" \
    "v_mfma_f32_16x16x32_bf16 v[64:67], v[48:51], a[16:19], v[64:67]\n" \
    "ds_read_b128 v[40:43], v33 offset:6144\n" \
    "v_mfma_f32_16x16x32_bf16 v[72:75], v[48:51], a[72:75], v[72:75]\n" \
    "v_cvt_pk_bf16_f32 v140, v120, v121\n" \
    "v_cvt_pk_bf16_f32 v141, v122, v123\n" \
    "v_mfma_f32_16x16x32_bf16 v[80:83], v[48:51], a[128:131], v[80:83]\n" \
    "v_mfma_f32_16x16x32_bf16 v[88:91], v[48:51], a[184:187], v[88:91]\n" \
    "v_cvt_pk_bf16_f32 v142, v124, v125\n" \
    "v_cvt_pk_bf16_f32 v143, v126, v127\n" \
    "s_waitcnt lgkmcnt(3)\n" \
    "v_mfma_f32_16x16x32_bf16 v[68:71], v[52:55], a[16:19], v[68:71]\n" \
    "ds_read_b128 v[44:47], v33 offset:20480\n" \
    "v_mfma_f32_16x16x32_bf16 v[76:79], v[52:55], a[72:75], v[76:79]\n" \
    "buffer_store_dwordx4 v[140:143], v36, s[48:51], s53 offen offset:3072 sc1\n" \
    "v_mfma_f32_16x16x32_bf16 v[84:87], v[52:55], a[128:131], v[84:87]\n" \
    "v_mfma_f32_16x16x32_bf16 v[92:95], v[52:55], a[184:187], v[92:95]\n" \
    "s_waitcnt lgkmcnt(3)\n" \
    "v_mfma_f32_16x16x32_bf16 v[64:67], v[56:59], a[20:23], v[64:67]\n" \
    "ds_read_b128 v[48:51], v33 offset:7168\n" \
    "v_mfma_f32_16x16x32_bf16 v[72:75], v[56:59], a[76:79], v[72:75]\n" \
    "v_mfma_f32_16x16x32_bf16 v[80:83], v[56:59], a[132:135], v[80:83]\n" \
    "v_mfma_f32_16x16x32_bf16 v[88:91], v[56:59], a[188:191], v[88:91]\n" \
    "s_waitcnt lgkmcnt(3)\n" \
    "v_mfma_f32_16x16x32_bf16 v[68:71], v[60:63], a[20:23], v[68:71]\n" \
    "ds_read_b128 v[52:55], v33 offset:21504\n" \
    "v_mfma_f32_16x16x32_bf16 v[76:79], v[60:63], a[76:79], v[76:79]\n" \
    "v_mfma_f32_16x16x32_bf16 v[84:87], v[60:63], a[132:135], v[84:87]\n" \
    "v_mfma_f32_16x16x32_bf16 v[92:95], v[60:63], a[188:191], v[92:95]\n" \
    "s_waitcnt lgkmcnt(3)\n" \
    "v_mfma_f32_16x16x32_bf16 v[64:67], v[40:43], a[24:27], v[64:67]\n" \
    "ds_read_b128 v[56:59], v33 offset:8192\n" \
    "v_mfma_f32_16x16x32_bf16 v[72:75], v[40:43], a[80:83], v[72:75]\n" \
    "v_mfma_f32_16x16x32_bf16 v[80:83], v[40:43], a[136:139], v[80:83]\n" \
    "v_mfma_f32_16x16x32_bf16 v[88:91], v[40:43], a[192:195], v[88:91]\n" \
    "s_waitcnt lgkmcnt(3)\n" \
    "v_mfma_f32_16x16x32_bf16 v[68:71], v[44:47], a[24:27], v[68:71]\n" \
    "ds_read_b128 v[60:63], v33 offset:22528\n" \
    "v_mfma_f32_16x16x32_bf16 v[76:79], v[44:47], a[80:83], v[76:79]\n" \
    "v_mfma_f32_16x16x32_bf16 v[84:87], v[44:47], a[136:139], v[84:87]\n" \
    "v_mfma_f32_16x16x32_bf16 v[92:95], v[44:47], a[192:195], v[92:95]\n" \
    "s_waitcnt lgkmcnt(3)\n" \
    "v_mfma_f32_16x16x32_bf16 v[64:67], v[48:51], a[28:31], v[64:67]\n" \
    "ds_read_b128 v[40:43], v33 offset:9216\n" \
    "v_mfma_f32_16x16x32_bf16 v[72:75], v[48:51], a[84:87], v[72:75]\n" \
    "v_mfma_f32_16x16x32_bf16 v[80:83], v[48:51], a[140:143], v[80:83]\n" \
    "v_mfma_f32_16x16x32_bf16 v[88:91], v[48:51], a[196:199], v[88:91]\n" \
    "s_waitcnt lgkmcnt(3)\n" \
    "v_mfma_f32_16x16x32_bf16 v[68:71], v[52:55], a[28:31], v[68:71]\n" \
    "ds_read_b128 v[44:47], v33 offset:23552\n" \
    "v_mfma_f32_16x16x32_bf16 v[76:79], v[52:55], a[84:87], v[76:79]\n" \
    "v_mfma_f32_16x16x32_bf16 v[84:87], v[52:55], a[140:143], v[84:87]\n" \
    "v_mfma_f32_16x16x32_bf16 v[92:95], v[52:55], a[196:199], v[92:95]\n" \
    "s_waitcnt lgkmcnt(3)\n" \
    "v_mfma_f32_16x16x32_bf16 v[64:67], v[56:59], a[32:35], v[64:67]\n" \
    "ds_read_b128 v[48:51], v33 offset:10240\n" \
    "v_mfma_f32_16x16x32_bf16 v[72:75], v[56:59], a[88:91], v[72:75]\n" \
    "v_mfma_f32_16x16x32_bf16 v[80:83], v[56:59], a[144:147], v[80:83]\n" \
    "v_mfma_f32_16x16x32_bf16 v[88:91], v[56:59], a[200:203], v[88:91]\n" \
    "s_waitcnt lgkmcnt(3)\n" \
    "v_mfma_f32_16x16x32_bf16 v[68:71], v[60:63], a[32:35], v[68:71]\n" \
    "ds_read_b128 v[52:55], v33 offset:24576\n" \
    "v_mfma_f32_16x16x32_bf16 v[76:79], v[60:63], a[88:91], v[76:79]\n" \
    "v_mfma_f32_16x16x32_bf16 v[84:87], v[60:63], a[144:147], v[84:87]\n" \
    "v_mfma_f32_16x16x32_bf16 v[92:95], v[60:63], a[200:203], v[92:95]\n" \
    "s_waitcnt lgkmcnt(3)\n" \
    "v_mfma_f32_16x16x32_bf16 v[64:67], v[40:43], a[36:39], v[64:67]\n" \
    "ds_read_b128 v[56:59], v33 offset:11264\n" \
    "v_mfma_f32_16x16x32_bf16 v[72:75], v[40:43], a[92:95], v[72:75]\n" \
    "v_mfma_f32_16x16x32_bf16 v[80:83], v[40:43], a[148:151], v[80:83]\n" \
    "v_mfma_f32_16x16x32_bf16 v[88:91], v[40:43], a[204:207], v[88:91]\n" \
    "s_waitcnt lgkmcnt(3)\n" \
    "v_mfma_f32_16x16x32_bf16 v[68:71], v[44:47], a[36:39], v[68:71]\n" \
    "ds_read_b128 v[60:63], v33 offset:25600\n" \
    "v_mfma_f32_16x16x32_bf16 v[76:79], v[44:47], a[92:95], v[76:79]\n" \
    "v_mfma_f32_16x16x32_bf16 v[84:87], v[44:47], a[148:151], v[84:87]\n" \
    "v_mfma_f32_16x16x32_bf16 v[92:95], v[44:47], a[204:207], v[92:95]\n" \
    "s_waitcnt lgkmcnt(3)\n" \
    "v_mfma_f32_16x16x32_bf16 v[64:67], v[48:51], a[40:43], v[64:67]\n" \
    "ds_read_b128 v[40:43], v33 offset:12288\n" \
    "v_mfma_f32_16x16x32_bf16 v[72:75], v[48:51], a[96:99], v[72:75]\n" \
    "v_mfma_f32_16x16x32_bf16 v[80:83], v[48:51], a[152:155], v[80:83]\n" \
    "v_mfma_f32_16x16x32_bf16 v[88:91], v[48:51], a[208:211], v[88:91]\n" \
    "s_waitcnt lgkmcnt(3)\n" \
    "v_mfma_f32_16x16x32_bf16 v[68:71], v[52:55], a[40:43], v[68:71]\n" \
    "ds_read_b128 v[44:47], v33 offset:26624\n" \
    "v_mfma_f32_16x16x32_bf16 v[76:79], v[52:55], a[96:99], v[76:79]\n" \
    "v_mfma_f32_16x16x32_bf16 v[84:87], v[52:55], a[152:155], v[84:87]\n" \
    "v_mfma_f32_16x16x32_bf16 v[92:95], v[52:55], a[208:211], v[92:95]\n" \
    "s_waitcnt lgkmcnt(3)\n" \
    "v_mfma_f32_16x16x32_bf16 v[64:67], v[56:59], a[44:47], v[64:67]\n" \
    "ds_read_b128 v[48:51], v33 offset:13312\n" \
    "v_mfma_f32_16x16x32_bf16 v[72:75], v[56:59], a[100:103], v[72:75]\n" \
    "v_mfma_f32_16x16x32_bf16 v[80:83], v[56:59], a[156:159], v[80:83]\n" \
    "v_mfma_f32_16x16x32_bf16 v[88:91], v[56:59], a[212:215], v[88:91]\n" \
    "s_waitcnt lgkmcnt(3)\n" \
    "v_mfma_f32_16x16x32_bf16 v[68:71], v[60:63], a[44:47], v[68:71]\n" \
    "ds_read_b128 v[52:55], v33 offset:27648\n" \
    "v_mfma_f32_16x16x32_bf16 v[76:79], v[60:63], a[100:103], v[76:79]\n" \
    "v_mfma_f32_16x16x32_bf16 v[84:87], v[60:63], a[156:159], v[84:87]\n" \
    "v_mfma_f32_16x16x32_bf16 v[92:95], v[60:63], a[212:215], v[92:95]\n" \
    "s_waitcnt lgkmcnt(3)\n" \
    "v_mfma_f32_16x16x32_bf16 v[64:67], v[40:43], a[48:51], v[64:67]\n" \
    "v_mfma_f32_16x16x32_bf16 v[72:75], v[40:43], a[104:107], v[72:75]\n" \
    "v_mfma_f32_16x16x32_bf16 v[80:83], v[40:43], a[160:163], v[80:83]\n" \
    "v_mfma_f32_16x16x32_bf16 v[88:91], v[40:43], a[216:219], v[88:91]\n" \
    "s_waitcnt lgkmcnt(2)\n" \
    "v_mfma_f32_16x16x32_bf16 v[68:71], v[44:47], a[48:51], v[68:71]\n" \
    "v_mfma_f32_16x16x32_bf16 v[76:79], v[44:47], a[104:107], v[76:79]\n" \
    "v_mfma_f32_16x16x32_bf16 v[84:87], v[44:47], a[160:163], v[84:87]\n" \
    "v_mfma_f32_16x16x32_bf16 v[92:95], v[44:47], a[216:219], v[92:95]\n" \
    "s_waitcnt vmcnt(0)\n" \
    "s_waitcnt lgkmcnt(1)\n" \
    "v_mfma_f32_16x16x32_bf16 v[64:67], v[48:51], a[52:55], v[64:67]\n" \
    "v_mfma_f32_16x16x32_bf16 v[72:75], v[48:51], a[108:111], v[72:75]\n" \
    "v_mfma_f32_16x16x32_bf16 v[80:83], v[48:51], a[164:167], v[80:83]\n" \
    "v_mfma_f32_16x16x32_bf16 v[88:91], v[48:51], a[220:223], v[88:91]\n" \
    "s_waitcnt lgkmcnt(0)\n" \
    "v_mfma_f32_16x16x32_bf16 v[68:71], v[52:55], a[52:55], v[68:71]\n" \
    "v_mfma_f32_16x16x32_bf16 v[76:79], v[52:55], a[108:111], v[76:79]\n" \
    "v_mfma_f32_16x16x32_bf16 v[84:87], v[52:55], a[164:167], v[84:87]\n" \
    "v_mfma_f32_16x16x32_bf16 v[92:95], v[52:55], a[220:223], v[92:95]\n" \
    "v_xor_b32 v33, 0x8000, v33\n" \
    "v_mov_b32 v36, v32\n" \
    "s_add_u32 s53, s53, s63\n" \
    "s_cmp_gt_u32 s56, 3\n" \
    "s_cbranch_scc1 LG1_NE0%=\n" \
    "s_add_u32 s54, s53, 14336\n" \
    "s_nop 3\n" \
    "v_cvt_pk_bf16_f32 v128, v64, v65\n" \
    "v_cvt_pk_bf16_f32 v129, v66, v67\n" \
    "v_cvt_pk_bf16_f32 v130, v68, v69\n" \
    "v_cvt_pk_bf16_f32 v131, v70, v71\n" \
    "buffer_store_dwordx4 v[128:131], v36, s[48:51], s53 offen offset:0 sc1\n" \
    "v_cvt_pk_bf16_f32 v132, v72, v73\n" \
    "v_cvt_pk_bf16_f32 v133, v74, v75\n" \
    "v_cvt_pk_bf16_f32 v134, v76, v77\n" \
    "v_cvt_pk_bf16_f32 v135, v78, v79\n" \
    "buffer_store_dwordx4 v[132:135], v36, s[48:51], s53 offen offset:1024 sc1\n" \
    "v_cvt_pk_bf16_f32 v136, v80, v81\n" \
    "v_cvt_pk_bf16_f32 v137, v82, v83\n" \
    "v_cvt_pk_bf16_f32 v138, v84, v85\n" \
    "v_cvt_pk_bf16_f32 v139, v86, v87\n" \
    "buffer_store_dwordx4 v[136:139], v36, s[48:51], s53 offen offset:2048 sc1\n" \
    "v_cvt_pk_bf16_f32 v140, v88, v89\n" \
    "v_cvt_pk_bf16_f32 v141, v90, v91\n" \
    "v_cvt_pk_bf16_f32 v142, v92, v93\n" \
    "v_cvt_pk_bf16_f32 v143, v94, v95\n" \
    "buffer_store_dwordx4 v[140:143], v36, s[48:51], s53 offen offset:3072 sc1\n" \
    "s_waitcnt vmcnt(0)\n" \
    "s_barrier\n" \
    "s_cmp_eq_u32 %11, 0\n" \
    "s_cbranch_scc1 LG1_NE0%=\n" \
    "s_sub_u32 s55, s56, 0\n" \
    "s_mul_i32 s55, s55, %12\n" \
    "s_lshl_b32 s55, s55, 2\n" \
    "s_add_u32 s55, s55, s59\n" \
    "s_mov_b64 s[60:61], exec\n" \
    "s_mov_b64 exec, 1\n" \
    "buffer_atomic_add v35, v34, s[48:51], s55 offen\n" \
    "s_mov_b64 exec, s[60:61]\n" \
    "LG1_NE0%=:\n" \
    "s_add_u32 s56, s56, 1\n" \
    "s_barrier\n" \
    "ds_read_b128 v[40:43], v33 offset:0\n" \
    "ds_read_b128 v[44:47], v33 offset:14336\n" \
    "ds_read_b128 v[48:51], v33 offset:1024\n" \
    "ds_read_b128 v[52:55], v33 offset:15360\n" \
    "s_add_u32 s55, s56, 1\n" \
    "s_cmp_lt_u32 s55, %13\n" \
    "s_cselect_b32 s55, s62, 0\n" \
    "s_add_u32 s52, s52, s55\n" \
    "s_xor_b32 s57, s57, 0x8000\n" \
    "s_add_u32 s54, s53, 14336\n" \
    "s_cmp_lt_u32 s56, 6\n" \
    "s_cselect_b32 s55, 0, %11\n" \
    "s_cmp_eq_u32 s55, 0\n" \
    "s_cbranch_scc1 LG1_NS1%=\n" \
    "s_sub_u32 s55, s56, 2\n" \
    "s_mul_i32 s55, s55, %12\n" \
    "s_lshl_b32 s55, s55, 2\n" \
    "s_add_u32 s55, s55, s59\n" \
    "s_mov_b64 s[60:61], exec\n" \
    "s_mov_b64 exec, 1\n" \
    "buffer_atomic_add v35, v34, s[48:51], s55 offen\n" \
    "s_mov_b64 exec, s[60:61]\n" \
    "LG1_NS1%=:\n" \
    "s_waitcnt lgkmcnt(3)\n" \
    "v_mfma_f32_16x16x32_bf16 v[96:99], v[40:43], a[0:3], 0\n" \
    "ds_read_b128 v[56:59], v33 offset:2048\n" \
    "v_mfma_f32_16x16x32_bf16 v[104:107], v[40:43], a[56:59], 0\n" \
    "s_add_u32 m0, s57, 0\n" \
    "s_add_u32 s66, s52, 0\n" \
    "buffer_load_dwordx4 v32, s[48:51], s66 offen lds\n" \
    "v_mfma_f32_16x16x32_bf16 v[112:115], v[40:43], a[112:115], 0\n" \
    "v_mfma_f32_16x16x32_bf16 v[120:123], v[40:43], a[168:171], 0\n" \
    "buffer_load_dwordx4 v32, s[48:51], s66 offen offset:1024 lds\n" \
    "s_waitcnt lgkmcnt(3)\n" \
    "v_mfma_f32_16x16x32_bf16 v[100:103], v[44:47], a[0:3], 0\n" \
    "ds_read_b128 v[60:63], v33 offset:16384\n" \
    "v_mfma_f32_16x16x32_bf16 v[108:111], v[44:47], a[56:59], 0\n" \
    "buffer_load_dwordx4 v32, s[48:51], s66 offen offset:2048 lds\n" \
    "v_mfma_f32_16x16x32_bf16 v[116:119], v[44:47], a[112:115], 0\n" \
    "v_mfma_f32_16x16x32_bf16 v[124:127], v[44:47], a[168:171], 0\n" \
    "buffer_load_dwordx4 v32, s[48:51], s66 offen offset:3072 lds\n" \
    "s_waitcnt lgkmcnt(3)\n" \
    "v_mfma_f32_16x16x32_bf16 v[96:99], v[48:51], a[4:7], v[96:99]\n" \
    "ds_read_b128 v[40:43], v33 offset:3072\n" \
    "v_mfma_f32_16x16x32_bf16 v[104:107], v[48:51], a[60:63], v[104:107]\n" \
    "s_add_u32 m0, s57, 4096\n" \
    "s_add_u32 s66, s52, 4096\n" \
    "buffer_load_dwordx4 v32, s[48:51], s66 offen lds\n" \
    "v_mfma_f32_16x16x32_bf16 v[112:115], v[48:51], a[116:119], v[112:115]\n" \
    "v_mfma_f32_16x16x32_bf16 v[120:123], v[48:51], a[172:175], v[120:123]\n" \
    "buffer_load_dwordx4 v32, s[48:51], s66 offen offset:1024 lds\n" \
    "s_waitcnt lgkmcnt(3)\n" \
    "v_mfma_f32_16x16x32_bf16 v[100:103], v[52:55], a[4:7], v[100:103]\n" \
    "ds_read_b128 v[44:47], v33 offset:17408\n" \
    "v_mfma_f32_16x16x32_bf16 v[108:111], v[52:55], a[60:63], v[108:111]\n" \
    "buffer_load_dwordx4 v32, s[48:51], s66 offen offset:2048 lds\n" \
    "v_mfma_f32_16x16x32_bf16 v[116:119], v[52:55], a[116:119], v[116:119]\n" \
    "v_mfma_f32_16x16x32_bf16 v[124:127], v[52:55], a[172:175], v[124:127]\n" \
    "v_cvt_pk_bf16_f32 v128, v64, v65\n" \
    "v_cvt_pk_bf16_f32 v129, v66, v67\n" \
    "s_waitcnt lgkmcnt(3)\n" \
    "v_mfma_f32_16x16x32_bf16 v[96:99], v[56:59], a[8:11], v[96:99]\n" \
    "ds_read_b128 v[48:51], v33 offset:4096\n" \
    "v_mfma_f32_16x16x32_bf16 v[104:107], v[56:59], a[64:67], v[104:107]\n" \
    "v_cvt_pk_bf16_f32 v130, v68, v69\n" \
    "v_cvt_pk_bf16_f32 v131, v70, v71\n" \
    "v_mfma_f32_16x16x32_bf16 v[112:115], v[56:59], a[120:123], v[112:115]\n" \
    "v_mfma_f32_16x16x32_bf16 v[120:123], v[56:59], a[176:179], v[120:123]\n" \
    "buffer_store_dwordx4 v[128:131], v36, s[48:51], s53 offen offset:0 sc1\n" \
    "s_waitcnt lgkmcnt(3)\n" \
    "v_mfma_f32_16x16x32_bf16 v[100:103], v[60:63], a[8:11], v[100:103]\n" \
    "ds_read_b128 v[52:55], v33 offset:18432\n" \
    "v_mfma_f32_16x16x32_bf16 v[108:111], v[60:63], a[64:67], v[108:111]\n" \
    "v_cvt_pk_bf16_f32 v132, v72, v73\n" \
    "v_cvt_pk_bf16_f32 v133, v74, v75\n" \
    "v_mfma_f32_16x16x32_bf16 v[116:119], v[60:63], a[120:123], v[116:119]\n" \
    "v_mfma_f32_16x16x32_bf16 v[124:127], v[60:63], a[176:179], v[124:127]\n" \
    "v_cvt_pk_bf16_f32 v134, v76, v77\n" \
    "v_cvt_pk_bf16_f32 v135, v78, v79\n" \
    "s_waitcnt lgkmcnt(3)\n" \
    "v_mfma_f32_16x16x32_bf16 v[96:99], v[40:43], a[12:15], v[96:99]\n" \
    "ds_read_b128 v[56:59], v33 offset:5120\n" \
    "v_mfma_f32_16x16x32_bf16 v[104:107], v[40:43], a[68:71], v[104:107]\n" \
    "buffer_store_dwordx4 v[132:135], v36, s[48:51], s53 offen offset:1024 sc1\n" \
    "v_mfma_f32_16x16x32_bf16 v[112:115], v[40:43], a[124:127], v[112:115]\n" \
    "v_mfma_f32_16x16x32_bf16 v[120:123], v[40:43], a[180:183], v[120:123]\n" \
    "v_cvt_pk_bf16_f32 v136, v80, v81\n" \
    "v_cvt_pk_bf16_f32 v137, v82, v83\n" \
    "s_waitcnt lgkmcnt(3)\n" \
    "v_mfma_f32_16x16x32_bf16 v[100:103], v[44:47], a[12:15], v[100:103]\n" \
    "ds_read_b128 v[60:63], v33 offset:19456\n" \
    "v_mfma_f32_16x16x32_bf16 v[108:111], v[44:47], a[68:71], v[108:111]\n" \
    "v_cvt_pk_bf16_f32 v138, v84, v85\n" \
    "v_cvt_pk_bf16_f32 v139, v86, v87\n" \
    "v_mfma_f32_16x16x32_bf16 v[116:119], v[44:47], a[124:127], v[116:119]\n" \
    "v_mfma_f32_16x16x32_bf16 v[124:127], v[44:47], a[180:183], v[124:127]\n" \
    "buffer_store_dwordx4 v[136:139], v36, s[48:51], s53 offen offset:2048 sc1\n" \
    "s_waitcnt lgkmcnt(3)\n" \
    "v_mfma_f32_16x16x32_bf16 v[96:99], v[48:51], a[16:19], v[96:99]\n" \
    "ds_read_b128 v[40:43], v33 offset:6144\n" \
    "v_mfma_f32_16x16x32_bf16 v[104:107], v[48:51], a[72:75], v[104:107]\n" \
    "v_cvt_pk_bf16_f32 v140, v88, v89\n" \
    "v_cvt_pk_bf16_f32 v141, v90, v91\n" \
    "v_mfma_f32_16x16x32_bf16 v[112:115], v[48:51], a[128:131], v[112:115]\n" \
    "v_mfma_f32_16x16x32_bf16 v[120:123], v[48:51], a[184:187], v[120:123]\n" \
    "v_cvt_pk_bf16_f32 v142, v92, v93\n" \
    "v_cvt_pk_bf16_f32 v143, v94, v95\n" \
    "s_waitcnt lgkmcnt(3)\n" \
    "v_mfma_f32_16x16x32_bf16 v[100:103], v[52:55], a[16:19], v[100:103]\n" \
    "ds_read_b128 v[44:47], v33 offset:20480\n" \
    "v_mfma_f32_16x16x32_bf16 v[108:111], v[52:55], a[72:75], v[108:111]\n" \
    "buffer_store_dwordx4 v[140:143], v36, s[48:51], s53 offen offset:3072 sc1\n" \
    "v_mfma_f32_16x16x32_bf16 v[116:119], v[52:55], a[128:131], v[116:119]\n" \
    "v_mfma_f32_16x16x32_bf16 v[124:127], v[52:55], a[184:187], v[124:127]\n" \
    "s_waitcnt lgkmcnt(3)\n" \
    "v_mfma_f32_16x16x32_bf16 v[96:99], v[56:59], a[20:23], v[96:99]\n" \
    "ds_read_b128 v[48:51], v33 offset:7168\n" \
    "v_mfma_f32_16x16x32_bf16 v[104:107], v[56:59], a[76:79], v[104:107]\n" \
    "v_mfma_f32_16x16x32_bf16 v[112:115], v[56:59], a[132:135], v[112:115]\n" \
    "v_mfma_f32_16x16x32_bf16 v[120:123], v[56:59], a[188:191], v[120:123]\n" \
    "s_waitcnt lgkmcnt(3)\n" \
    "v_mfma_f32_16x16x32_bf16 v[100:103], v[60:63], a[20:23], v[100:103]\n" \
    "ds_read_b128 v[52:55], v33 offset:21504\n" \
    "v_mfma_f32_16x16x32_bf16 v[108:111], v[60:63], a[76:79], v[108:111]\n" \
    "v_mfma_f32_16x16x32_bf16 v[116:119], v[60:63], a[132:135], v[116:119]\n" \
    "v_mfma_f32_16x16x32_bf16 v[124:127], v[60:63], a[188:191], v[124:127]\n" \
    "s_waitcnt lgkmcnt(3)\n" \
    "v_mfma_f32_16x16x32_bf16 v[96:99], v[40:43], a[24:27], v[96:99]\n" \
    "ds_read_b128 v[56:59], v33 offset:8192\n" \
    "v_mfma_f32_16x16x32_bf16 v[104:107], v[40:43], a[80:83], v[104:107]\n" \
    "v_mfma_f32_16x16x32_bf16 v[112:115], v[40:43], a[136:139], v[112:115]\n" \
    "v_mfma_f32_16x16x32_bf16 v[120:123], v[40:43], a[192:195], v[120:123]\n" \
    "s_waitcnt lgkmcnt(3)\n" \
    "v_mfma_f32_16x16x32_bf16 v[100:103], v[44:47], a[24:27], v[100:103]\n" \
    "ds_read_b128 v[60:63], v33 offset:22528\n" \
    "v_mfma_f32_16x16x32_bf16 v[108:111], v[44:47], a[80:83], v[108:111]\n" \
    "v_mfma_f32_16x16x32_bf16 v[116:119], v[44:47], a[136:139], v[116:119]\n" \
    "v_mfma_f32_16x16x32_bf16 v[124:127], v[44:47], a[192:195], v[124:127]\n" \
    "s_waitcnt lgkmcnt(3)\n" \
    "v_mfma_f32_16x16x32_bf16 v[96:99], v[48:51], a[28:31], v[96:99]\n" \
    "ds_read_b128 v[40:43], v33 offset:9216\n" \
    "v_mfma_f32_16x16x32_bf16 v[104:107], v[48:51], a[84:87], v[104:107]\n" \
    "v_mfma_f32_16x16x32_bf16 v[112:115], v[48:51], a[140:143], v[112:115]\n" \
    "v_mfma_f32_16x16x32_bf16 v[120:123], v[48:51], a[196:199], v[120:123]\n" \
    "s_waitcnt lgkmcnt(3)\n" \
    "v_mfma_f32_16x16x32_bf16 v[100:103], v[52:55], a[28:31], v[100:103]\n" \
    "ds_read_b128 v[44:47], v33 offset:23552\n" \
    "v_mfma_f32_16x16x32_bf16 v[108:111], v[52:55], a[84:87], v[108:111]\n" \
    "v_mfma_f32_16x16x32_bf16 v[116:119], v[52:55], a[140:143], v[116:119]\n" \
    "v_mfma_f32_16x16x32_bf16 v[124:127], v[52:55], a[196:199], v[124:127]\n" \
    "s_waitcnt lgkmcnt(3)\n" \
    "v_mfma_f32_16x16x32_bf16 v[96:99], v[56:59], a[32:35], v[96:99]\n" \
    "ds_read_b128 v[48:51], v33 offset:10240\n" \
    "v_mfma_f32_16x16x32_bf16 v[104:107], v[56:59], a[88:91], v[104:107]\n" \
    "v_mfma_f32_16x16x32_bf16 v[112:115], v[56:59], a[144:147], v[112:115]\n" \
    "v_mfma_f32_16x16x32_bf16 v[120:123], v[56:59], a[200:203], v[120:123]\n" \
    "s_waitcnt lgkmcnt(3)\n" \
    "v_mfma_f32_16x16x32_bf16 v[100:103], v[60:63], a[32:35], v[100:103]\n" \
    "ds_read_b128 v[52:55], v33 offset:24576\n" \
    "v_mfma_f32_16x16x32_bf16 v[108:111], v[60:63], a[88:91], v[108:111]\n" \
    "v_mfma_f32_16x16x32_bf16 v[116:119], v[60:63], a[144:147], v[116:119]\n" \
    "v_mfma_f32_16x16x32_bf16 v[124:127], v[60:63], a[200:203], v[124:127]\n" \
    "s_waitcnt lgkmcnt(3)\n" \
    "v_mfma_f32_16x16x32_bf16 v[96:99], v[40:43], a[36:39], v[96:99]\n" \
    "ds_read_b128 v[56:59], v33 offset:11264\n" \
    "v_mfma_f32_16x16x32_bf16 v[104:107], v[40:43], a[92:95], v[104:107]\n" \
    "v_mfma_f32_16x16x32_bf16 v[112:115], v[40:43], a[148:151], v[112:115]\n" \
    "v_mfma_f32_16x16x32_bf16 v[120:123], v[40:43], a[204:207], v[120:123]\n" \
    "s_waitcnt lgkmcnt(3)\n" \
    "v_mfma_f32_16x16x32_bf16 v[100:103], v[44:47], a[36:39], v[100:103]\n" \
    "ds_read_b128 v[60:63], v33 offset:25600\n" \
    "v_mfma_f32_16x16x32_bf16 v[108:111], v[44:47], a[92:95], v[108:111]\n" \
    "v_mfma_f32_16x16x32_bf16 v[116:119], v[44:47], a[148:151], v[116:119]\n" \
    "v_mfma_f32_16x16x32_bf16 v[124:127], v[44:47], a[204:207], v[124:127]\n" \
    "s_waitcnt lgkmcnt(3)\n" \
    "v_mfma_f32_16x16x32_bf16 v[96:99], v[48:51], a[40:43], v[96:99]\n" \
    "ds_read_b128 v[40:43], v33 offset:12288\n" \
    "v_mfma_f32_16x16x32_bf16 v[104:107], v[48:51], a[96:99], v[104:107]\n" \
    "v_mfma_f32_16x16x32_bf16 v[112:115], v[48:51], a[152:155], v[112:115]\n" \
    "v_mfma_f32_16x16x32_bf16 v[120:123], v[48:51], a[208:211], v[120:123]\n" \
    "s_waitcnt lgkmcnt(3)\n" \
    "v_mfma_f32_16x16x32_bf16 v[100:103], v[52:55], a[40:43], v[100:103]\n" \
    "ds_read_b128 v[44:47], v33 offset:26624\n" \
    "v_mfma_f32_16x16x32_bf16 v[108:111], v[52:55], a[96:99], v[108:111]\n" \
    "v_mfma_f32_16x16x32_bf16 v[116:119], v[52:55], a[152:155], v[116:119]\n" \
    "v_mfma_f32_16x16x32_bf16 v[124:127], v[52:55], a[208:211], v[124:127]\n" \
    "s_waitcnt lgkmcnt(3)\n" \
    "v_mfma_f32_16x16x32_bf16 v[96:99], v[56:59], a[44:47], v[96:99]\n" \
    "ds_read_b128 v[48:51], v33 offset:13312\n" \
    "v_mfma_f32_16x16x32_bf16 v[104:107], v[56:59], a[100:103], v[104:107]\n" \
    "v_mfma_f32_16x16x32_bf16 v[112:115], v[56:59], a[156:159], v[112:115]\n" \
    "v_mfma_f32_16x16x32_bf16 v[120:123], v[56:59], a[212:215], v[120:123]\n" \
    "s_waitcnt lgkmcnt(3)\n" \
    "v_mfma_f32_16x16x32_bf16 v[100:103], v[60:63], a[44:47], v[100:103]\n" \
    "ds_read_b128 v[52:55], v33 offset:27648\n" \
    "v_mfma_f32_16x16x32_bf16 v[108:111], v[60:63], a[100:103], v[108:111]\n" \
    "v_mfma_f32_16x16x32_bf16 v[116:119], v[60:63], a[156:159], v[116:119]\n" \
    "v_mfma_f32_16x16x32_bf16 v[124:127], v[60:63], a[212:215], v[124:127]\n" \
    "s_waitcnt lgkmcnt(3)\n" \
    "v_mfma_f32_16x16x32_bf16 v[96:99], v[40:43], a[48:51], v[96:99]\n" \
    "v_mfma_f32_16x16x32_bf16 v[104:107], v[40:43], a[104:107], v[104:107]\n" \
    "v_mfma_f32_16x16x32_bf16 v[112:115], v[40:43], a[160:163], v[112:115]\n" \
    "v_mfma_f32_16x16x32_bf16 v[120:123], v[40:43], a[216:219], v[120:123]\n" \
    "s_waitcnt lgkmcnt(2)\n" \
    "v_mfma_f32_16x16x32_bf16 v[100:103], v[44:47], a[48:51], v[100:103]\n" \
    "v_mfma_f32_16x16x32_bf16 v[108:111], v[44:47], a[104:107], v[108:111]\n" \
    "v_mfma_f32_16x16x32_bf16 v[116:119], v[44:47], a[160:163], v[116:119]\n" \
    "v_mfma_f32_16x16x32_bf16 v[124:127], v[44:47], a[216:219], v[124:127]\n" \
    "s_waitcnt vmcnt(0)\n" \
    "s_waitcnt lgkmcnt(1)\n" \
    "v_mfma_f32_16x16x32_bf16 v[96:99], v[48:51], a[52:55], v[96:99]\n" \
    "v_mfma_f32_16x16x32_bf16 v[104:107], v[48:51], a[108:111], v[104:107]\n" \
    "v_mfma_f32_16x16x32_bf16 v[112:115], v[48:51], a[164:167], v[112:115]\n" \
    "v_mfma_f32_16x16x32_bf16 v[120:123], v[48:51], a[220:223], v[120:123]\n" \
    "s_waitcnt lgkmcnt(0)\n" \
    "v_mfma_f32_16x16x32_bf16 v[100:103], v[52:55], a[52:55], v[100:103]\n" \
    "v_mfma_f32_16x16x32_bf16 v[108:111], v[52:55], a[108:111], v[108:111]\n" \
    "v_mfma_f32_16x16x32_bf16 v[116:119], v[52:55], a[164:167], v[116:119]\n" \
    "v_mfma_f32_16x16x32_bf16 v[124:127], v[52:55], a[220:223], v[124:127]\n" \
    "v_xor_b32 v33, 0x8000, v33\n" \
    "v_mov_b32 v36, v32\n" \
    "s_add_u32 s53, s53, s63\n" \
    "s_cmp_gt_u32 s56, 3\n" \
    "s_cbranch_scc1 LG1_NE1%=\n" \
    "s_add_u32 s54, s53, 14336\n" \
    "s_nop 3\n" \
    "v_cvt_pk_bf16_f32 v128, v96, v97\n" \
    "v_cvt_pk_bf16_f32 v129, v98, v99\n" \
    "v_cvt_pk_bf16_f32 v130, v100, v101\n" \
    "v_cvt_pk_bf16_f32 v131, v102, v103\n" \
    "buffer_store_dwordx4 v[128:131], v36, s[48:51], s53 offen offset:0 sc1\n" \
    "v_cvt_pk_bf16_f32 v132, v104, v105\n" \
    "v_cvt_pk_bf16_f32 v133, v106, v107\n" \
    "v_cvt_pk_bf16_f32 v134, v108, v109\n" \
    "v_cvt_pk_bf16_f32 v135, v110, v111\n" \
    "buffer_store_dwordx4 v[132:135], v36, s[48:51], s53 offen offset:1024 sc1\n" \
    "v_cvt_pk_bf16_f32 v136, v112, v113\n" \
    "v_cvt_pk_bf16_f32 v137, v114, v115\n" \
    "v_cvt_pk_bf16_f32 v138, v116, v117\n" \
    "v_cvt_pk_bf16_f32 v139, v118, v119\n" \
    "buffer_store_dwordx4 v[136:139], v36, s[48:51], s53 offen offset:2048 sc1\n" \
    "v_cvt_pk_bf16_f32 v140, v120, v121\n" \
    "v_cvt_pk_bf16_f32 v141, v122, v123\n" \
    "v_cvt_pk_bf16_f32 v142, v124, v125\n" \
    "v_cvt_pk_bf16_f32 v143, v126, v127\n" \
    "buffer_store_dwordx4 v[140:143], v36, s[48:51], s53 offen offset:3072 sc1\n" \
    "s_waitcnt vmcnt(0)\n" \
    "s_barrier\n" \
    "s_cmp_eq_u32 %11, 0\n" \
    "s_cbranch_scc1 LG1_NE1%=\n" \
    "s_sub_u32 s55, s56, 0\n" \
    "s_mul_i32 s55, s55, %12\n" \
    "s_lshl_b32 s55, s55, 2\n" \
    "s_add_u32 s55, s55, s59\n" \
    "s_mov_b64 s[60:61], exec\n" \
    "s_mov_b64 exec, 1\n" \
    "buffer_atomic_add v35, v34, s[48:51], s55 offen\n" \
    "s_mov_b64 exec, s[60:61]\n" \
    "LG1_NE1%=:\n" \
    "s_add_u32 s56, s56, 1\n" \
    "s_cmp_lt_u32 s56, %13\n" \
    "s_cbranch_scc1 LG1_LOOP%=\n" \
    "s_add_u32 s54, s53, 14336\n" \
    "s_nop 3\n" \
    "v_cvt_pk_bf16_f32 v128, v96, v97\n" \
    "v_cvt_pk_bf16_f32 v129, v98, v99\n" \
    "v_cvt_pk_bf16_f32 v130, v100, v101\n" \
    "v_cvt_pk_bf16_f32 v131, v102, v103\n" \
    "buffer_store_dwordx4 v[128:131], v36, s[48:51], s53 offen offset:0 sc1\n" \
    "v_cvt_pk_bf16_f32 v132, v104, v105\n" \
    "v_cvt_pk_bf16_f32 v133, v106, v107\n" \
    "v_cvt_pk_bf16_f32 v134, v108, v109\n" \
    "v_cvt_pk_bf16_f32 v135, v110, v111\n" \
    "buffer_store_dwordx4 v[132:135], v36, s[48:51], s53 offen offset:1024 sc1\n" \
    "v_cvt_pk_bf16_f32 v136, v112, v113\n" \
    "v_cvt_pk_bf16_f32 v137, v114, v115\n" \
    "v_cvt_pk_bf16_f32 v138, v116, v117\n" \
    "v_cvt_pk_bf16_f32 v139, v118, v119\n" \
    "buffer_store_dwordx4 v[136:139], v36, s[48:51], s53 offen offset:2048 sc1\n" \
    "v_cvt_pk_bf16_f32 v140, v120, v121\n" \
    "v_cvt_pk_bf16_f32 v141, v122, v123\n" \
    "v_cvt_pk_bf16_f32 v142, v124, v125\n" \
    "v_cvt_pk_bf16_f32 v143, v126, v127\n" \
    "buffer_store_dwordx4 v[140:143], v36, s[48:51], s53 offen offset:3072 sc1\n" \
    "s_waitcnt vmcnt(0)\n" \
    "s_barrier\n" \
    "s_cmp_eq_u32 %11, 0\n" \
    "s_cbranch_scc1 LG1_END%=\n" \
    "s_sub_u32 s55, s56, 2\n" \
    "s_mul_i32 s55, s55, %12\n" \
    "s_lshl_b32 s55, s55, 2\n" \
    "s_add_u32 s55, s55, s59\n" \
    "s_mov_b64 s[60:61], exec\n" \
    "s_mov_b64 exec, 1\n" \
    "buffer_atomic_add v35, v34, s[48:51], s55 offen\n" \
    "s_mov_b64 exec, s[60:61]\n" \
    "s_sub_u32 s55, s56, 1\n" \
    "s_mul_i32 s55, s55, %12\n" \
    "s_lshl_b32 s55, s55, 2\n" \
    "s_add_u32 s55, s55, s59\n" \
    "s_mov_b64 s[60:61], exec\n" \
    "s_mov_b64 exec, 1\n" \
    "buffer_atomic_add v35, v34, s[48:51], s55 offen\n" \
    "s_mov_b64 exec, s[60:61]\n" \
    "LG1_END%=:\n" \
    "s_mov_b32 m0, s58\n" \
    "s_waitcnt vmcnt(0)\n"
#define CLOB_GEMM \
    "v32", "v33", "v34", "v35", "v36", "v37", "v38", "v39", "v40", "v41", "v42", "v43", "v44", "v45", "v46", "v47", "v48", "v49", "v50", "v51", "v52",  \
    "v53", "v54", "v55", "v56", "v57", "v58", "v59", "v60", "v61", "v62", "v63", "v64", "v65", "v66", "v67", "v68", "v69", "v70", "v71", "v72", "v73",  \
    "v74", "v75", "v76", "v77", "v78", "v79", "v80", "v81", "v82", "v83", "v84", "v85", "v86", "v87", "v88", "v89", "v90", "v91", "v92", "v93", "v94",  \
    "v95", "v96", "v97", "v98", "v99", "v100", "v101", "v102", "v103", "v104", "v105", "v106", "v107", "v108", "v109", "v110", "v111", "v112", "v113",  \
    "v114", "v115", "v116", "v117", "v118", "v119", "v120", "v121", "v122", "v123", "v124", "v125", "v126", "v127", "v128", "v129", "v130", "v131",  \
    "v132", "v133", "v134", "v135", "v136", "v137", "v138", "v139", "v140", "v141", "v142", "v143", "a0", "a1", "a2", "a3", "a4", "a5", "a6", "a7",  \
    "a8", "a9", "a10", "a11", "a12", "a13", "a14", "a15", "a16", "a17", "a18", "a19", "a20", "a21", "a22", "a23", "a24", "a25", "a26", "a27", "a28",  \
    "a29", "a30", "a31", "a32", "a33", "a34", "a35", "a36", "a37", "a38", "a39", "a40", "a41", "a42", "a43", "a44", "a45", "a46", "a47", "a48", "a49",  \
    "a50", "a51", "a52", "a53", "a54", "a55", "a56", "a57", "a58", "a59", "a60", "a61", "a62", "a63", "a64", "a65", "a66", "a67", "a68", "a69", "a70",  \
    "a71", "a72", "a73", "a74", "a75", "a76", "a77", "a78", "a79", "a80", "a81", "a82", "a83", "a84", "a85", "a86", "a87", "a88", "a89", "a90", "a91",  \
    "a92", "a93", "a94", "a95", "a96", "a97", "a98", "a99", "a100", "a101", "a102", "a103", "a104", "a105", "a106", "a107", "a108", "a109", "a110",  \
    "a111", "a112", "a113", "a114", "a115", "a116", "a117", "a118", "a119", "a120", "a121", "a122", "a123", "a124", "a125", "a126", "a127", "a128",  \
    "a129", "a130", "a131", "a132", "a133", "a134", "a135", "a136", "a137", "a138", "a139", "a140", "a141", "a142", "a143", "a144", "a145", "a146",  \
    "a147", "a148", "a149", "a150", "a151", "a152", "a153", "a154", "a155", "a156", "a157", "a158", "a159", "a160", "a161", "a162", "a163", "a164",  \
    "a165", "a166", "a167", "a168", "a169", "a170", "a171", "a172", "a173", "a174", "a175", "a176", "a177", "a178", "a179", "a180", "a181", "a182",  \
    "a183", "a184", "a185", "a186", "a187", "a188", "a189", "a190", "a191", "a192", "a193", "a194", "a195", "a196", "a197", "a198", "a199", "a200",  \
    "a201", "a202", "a203", "a204", "a205", "a206", "a207", "a208", "a209", "a210", "a211", "a212", "a213", "a214", "a215", "a216", "a217", "a218",  \
    "a219", "a220", "a221", "a222", "a223", "s40", "s41", "s42", "s43", "s44", "s45", "s46", "s47", "s48", "s49", "s50", "s51", "s52", "s53", "s54",  \
    "s55", "s56", "s57", "s58", "s59", "s60", "s61", "s62", "s63", "s64", "s65", "s66", "s67", "s68", "s69", "s70", "s71", "s72", "s73", "s74", "vcc",  \
    "scc", "memory"
#define CLOB_SCORE \
    "v32", "v33", "v34", "v35", "v36", "v37", "v38", "v39", "v40", "v41", "v42", "v43", "v44", "v45", "v46", "v47", "v48", "v49", "v50", "v51", "v52",  \
    "v53", "v54", "v55", "v56", "v57", "v58", "v59", "v60", "v61", "v62", "v63", "v64", "v65", "v66", "v67", "v68", "v69", "v70", "v71", "v72", "v73",  \
    "v74", "v75", "v76", "v77", "v78", "v79", "v80", "v81", "v82", "v83", "v84", "v85", "v86", "v87", "v88", "v89", "v90", "v91", "v92", "v93", "v94",  \
    "v95", "v96", "v97", "v98", "v99", "v100", "v101", "v102", "v103", "v104", "v105", "v106", "v107", "v108", "v109", "v110", "v111", "v112", "v113",  \
    "v114", "v115", "v116", "v117", "v118", "v119", "v120", "v121", "v122", "v123", "v124", "v125", "v126", "v127", "v128", "v129", "v130", "v131",  \
    "v132", "v133", "v134", "v135", "v136", "v137", "v138", "v139", "v140", "v141", "v142", "v143", "v144", "v145", "v146", "v147", "v148", "v149",  \
    "v150", "v151", "v152", "v153", "v154", "v155", "v156", "v157", "v158", "v159", "v160", "v161", "v162", "v163", "v164", "v165", "v166", "v167",  \
    "v168", "v169", "v170", "v171", "v172", "v173", "v174", "v175", "v176", "v177", "v178", "v179", "v180", "v181", "v182", "v183", "a0", "a1", "a2",  \
    "a3", "a4", "a5", "a6", "a7", "a8", "a9", "a10", "a11", "a12", "a13", "a14", "a15", "a16", "a17", "a18", "a19", "a20", "a21", "a22", "a23", "a24",  \
    "a25", "a26", "a27", "a28", "a29", "a30", "a31", "a32", "a33", "a34", "a35", "a36", "a37", "a38", "a39", "a40", "a41", "a42", "a43", "a44", "a45",  \
    "a46", "a47", "a48", "a49", "a50", "a51", "a52", "a53", "a54", "a55", "a56", "a57", "a58", "a59", "a60", "a61", "a62", "a63", "a64", "a65", "a66",  \
    "a67", "a68", "a69", "a70", "a71", "a72", "a73", "a74", "a75", "a76", "a77", "a78", "a79", "a80", "a81", "a82", "a83", "a84", "a85", "a86", "a87",  \
    "a88", "a89", "a90", "a91", "a92", "a93", "a94", "a95", "a96", "a97", "a98", "a99", "a100", "a101", "a102", "a103", "a104", "a105", "a106", "a107",  \
    "a108", "a109", "a110", "a111", "s40", "s41", "s42", "s43", "s44", "s45", "s46", "s47", "s48", "s49", "s50", "s51", "s52", "s53", "s54", "s55",  \
    "s56", "s57", "s58", "s59", "s60", "s61", "s62", "s63", "s64", "s65", "s66", "s67", "s68", "s69", "s70", "s71", "s72", "s73", "s74", "vcc", "scc",  \
    "memory"
#define CLOB_OUT \
    "v32", "v33", "v34", "v35", "v36", "v37", "v38", "v39", "v40", "v41", "v42", "v43", "v44", "v45", "v46", "v47", "v48", "v49", "v50", "v51", "v52",  \
    "v53", "v54", "v55", "v56", "v57", "v58", "v59", "v60", "v61", "v62", "v63", "v64", "v65", "v66", "v67", "v68", "v69", "v70", "v71", "v72", "v73",  \
    "v74", "v75", "v76", "v77", "v78", "v79", "v80", "v81", "v82", "v83", "v84", "v85", "v86", "v87", "v88", "v89", "v90", "v91", "v92", "v93", "v94",  \
    "v95", "v96", "v97", "v98", "v99", "v100", "v101", "v102", "v103", "v104", "v105", "v106", "v107", "v108", "v109", "v110", "v111", "v112", "v113",  \
    "v114", "v115", "v116", "v117", "v118", "v119", "v120", "v121", "v122", "v123", "v124", "v125", "v126", "v127", "v128", "v129", "v130", "v131",  \
    "v132", "v133", "v134", "v135", "v136", "v137", "v138", "v139", "v140", "v141", "v142", "v143", "v144", "v145", "v146", "v147", "v148", "v149",  \
    "v150", "v151", "v152", "v153", "v154", "v155", "v156", "v157", "v158", "v159", "v160", "v161", "v162", "v163", "v164", "v165", "v166", "v167",  \
    "v168", "v169", "v170", "v171", "v172", "v173", "v174", "v175", "v176", "v177", "v178", "v179", "v180", "v181", "v182", "v183", "a0", "a1", "a2",  \
    "a3", "a4", "a5", "a6", "a7", "a8", "a9", "a10", "a11", "a12", "a13", "a14", "a15", "a16", "a17", "a18", "a19", "a20", "a21", "a22", "a23", "a24",  \
    "a25", "a26", "a27", "a28", "a29", "a30", "a31", "a32", "a33", "a34", "a35", "a36", "a37", "a38", "a39", "a40", "a41", "a42", "a43", "a44", "a45",  \
    "a46", "a47", "a48", "a49", "a50", "a51", "a52", "a53", "a54", "a55", "a56", "a57", "a58", "a59", "a60", "a61", "a62", "a63", "a64", "a65", "a66",  \
    "a67", "a68", "a69", "a70", "a71", "a72", "a73", "a74", "a75", "a76", "a77", "a78", "a79", "a80", "a81", "a82", "a83", "a84", "a85", "a86", "a87",  \
    "a88", "a89", "a90", "a91", "a92", "a93", "a94", "a95", "a96", "a97", "a98", "a99", "a100", "a101", "a102", "a103", "a104", "a105", "a106", "a107",  \
    "a108", "a109", "a110", "a111", "a112", "a113", "a114", "a115", "a116", "a117", "a118", "a119", "a120", "a121", "a122", "a123", "a124", "a125",  \
    "a126", "a127", "a128", "a129", "a130", "a131", "a132", "a133", "a134", "a135", "a136", "a137", "a138", "a139", "a140", "a141", "a142", "a143",  \
    "a144", "a145", "a146", "a147", "a148", "a149", "a150", "a151", "a152", "a153", "a154", "a155", "a156", "a157", "a158", "a159", "a160", "a161",  \
    "a162", "a163", "a164", "a165", "a166", "a167", "a168", "a169", "a170", "a171", "a172", "a173", "a174", "a175", "a176", "a177", "a178", "a179",  \
    "a180", "a181", "a182", "a183", "a184", "a185", "a186", "a187", "a188", "a189", "a190", "a191", "a192", "a193", "a194", "a195", "a196", "a197",  \
    "a198", "a199", "a200", "a201", "a202", "a203", "a204", "a205", "a206", "a207", "a208", "a209", "a210", "a211", "a212", "a213", "a214", "a215",  \
    "a216", "a217", "a218", "a219", "a220", "a221", "a222", "a223", "s40", "s41", "s42", "s43", "s44", "s45", "s46", "s47", "s48", "s49", "s50", "s51",  \
    "s52", "s53", "s54", "s55", "s56", "s57", "s58", "s59", "s60", "s61", "s62", "s63", "s64", "s65", "s66", "s67", "s68", "s69", "s70", "s71", "s72",  \
    "s73", "s74", "vcc", "scc", "memory"

constexpr unsigned OOR = 0x7fffff00u;
#define RSRC_OPS "s"(c0), "s"(c1), "s"(c2), "s"(c3), "s"(w0), "s"(w1), "s"(w2), "s"(w3)
#define RSRC_DECL                                                                                                                      \
    const unsigned c0 = (unsigned)(size_t)p.comm, c1 = (unsigned)((size_t)p.comm >> 32) & 0xffffu, c2 = (unsigned)COMM_BYTES, c3 = 0x00020000u; \
    const unsigned w0 = (unsigned)(size_t)p.ws, w1 = (unsigned)((size_t)p.ws >> 32) & 0xffffu, w2 = (unsigned)OFF_END, w3 = 0x00020000u;
#define DG_OUT

#define COMMON_OPS "v"(ca), RSRC_OPS, "s"(p.d_score_k), "s"(p.d_score), "s"(p.d_score_s), "s"(p.d_outmem0), "s"(k0)
DI void score_wave(const Params& p, char* smem, int h, int q, int xg, int wave, int lane) {
    const int r16 = lane & 15, g = lane >> 4, tile = q & 1, ch = q >> 1;
    const unsigned aoff = g * 256 + ((r16 >> 2) & 1) * 128 + (r16 >> 3) * 64 + (r16 & 3) * 16;
    const int k0 = (2 * wave) % 7, k1 = (2 * wave + 1) % 7;
    const unsigned sb = (unsigned)(size_t)smem;
    const unsigned MEML = sb, LT = sb + 8192, EL = sb + 12288;
    unsigned* cst = (unsigned*)(smem + 16384 + wave * 9216) + lane;
    cst[0 * 64] = xg * CPY + k0 * 1024 + aoff;
    cst[1 * 64] = xg * CPY + (wave < 3 ? k1 * 1024 + aoff : 7168);
    cst[2 * 64] = MEML + k0 * 1024 + lane * 16;
    cst[3 * 64] = MEML + k1 * 1024 + lane * 16;
#pragma unroll
    for (int sl = 2; sl < 7; ++sl) cst[(2 + sl) * 64] = MEML + ((2 * wave + sl) % 7) * 1024 + lane * 16;
    const int tau0 = (7 * q) & ~1;
#pragma unroll
    for (int sig = 0; sig < 2; ++sig) { const int tau = tau0 + 4 * sig + g; cst[(9 + sig) * 64] = MEML + (tau >> 2) * 1024 + ((tau & 3) * 16 + r16) * 16; }
    {
#pragma unroll
        for (int k = 0; k < 2; ++k) {
            const int tl = 7 * q + 2 * wave + k - tau0;
            const int row = 32 * (tl >> 1) + 8 * (r16 >> 2) + 4 * (tl & 1) + (r16 & 3);
            cst[(11 + k) * 64] = LT + (row ^ ((row & 8) >> 1)) * 32 + 8 * (g ^ ((row >> 3) & 3));
        }
#pragma unroll
        for (int v = 0; v < 2; ++v) {
            const int row = 8 * g + 4 * v + (r16 >> 2);
            cst[(v ? 18 : 13) * 64] = LT + (row ^ ((row & 8) >> 1)) * 32 + 8 * ((r16 & 3) ^ ((row >> 3) & 3));
        }
    }
    cst[14 * 64] = (2 * (g & 1) + (r16 >> 3)) * CPY + 16384 + (h * 4 + q) * 128 + (2 * (g >> 1) + (r16 & 1)) * 32 + ((r16 & 7) >> 1) * 8;
    cst[15 * 64] = (r16 & 8) ? 0xEEEE : 0x4444;
    cst[17 * 64] = lane * 16;
    cst[19 * 64] = MEML;
    cst[20 * 64] = 1024 + lane * 4;
    cst[21 * 64] = (wave == 0 && lane == 0) ? CTL_RDY * 4 : OOR;
    cst[22 * 64] = wave == 3 ? EL + (4 * (g & 1) + (g >> 1)) * 32 + r16 * 2 : EL + (r16 & 7) * 32 + g * 8;
    cst[23 * 64] = wave == 3 ? (r16 == 0 ? (2 * (g & 1) + ch) * CPY + 32768 + (h * 2 + tile) * 128 + (g >> 1) * 8 : OOR)
                             : ((r16 == 0 && g < 2) ? (2 * g + ch) * CPY + 32768 + (h * 2 + tile) * 128 + 64 : OOR);
    cst[24 * 64] = lane * 16 + tile * 8;
#pragma unroll
    for (int i = 0; i < 3; ++i) {
        const int tl = 2 * i + (g >> 1), csl = 5 * wave + tl;
        cst[(25 + i) * 64] = (tl < 5 && csl < 14) ? (2 * (g & 1) + ch) * CPY + (3 + (csl >> 1)) * 16384 + (csl & 1) * 4096 + h * 256 + tile * 128 + r16 * 8 : OOR;
    }
    RSRC_DECL
    const unsigned ca = sb + 16384 + wave * 9216 + lane * 4;
    const unsigned offA = (unsigned)(OFF_WB + (size_t)(h * NT + 7 * q + 2 * wave) * TILE_B);
    const unsigned offK = (unsigned)(OFF_KM + (size_t)(h * 4 + q) * 4096);
    const unsigned offS = wave == 3 ? (unsigned)(OFF_KQ + (size_t)(h * 2 + tile) * TILE_B) : (unsigned)(OFF_VO + (size_t)(h * NT + 14 * ch + 5 * wave) * 1024);
    asm volatile("s_waitcnt lgkmcnt(0)" ::: "memory");
    if (wave < 2) asm volatile(ASM_SCORE0 ::COMMON_OPS, "s"(offA), "s"(offS), "s"(p.pad_), "s"(offK) : CLOB_SCORE);
    else if (wave == 2) asm volatile(ASM_SCORE2 ::COMMON_OPS, "s"(offA), "s"(offS), "s"(p.pad_), "s"(offK) : CLOB_SCORE);
    else asm volatile(ASM_SCORE3 ::COMMON_OPS, "s"(offA), "s"(offS), "s"(p.pad_), "s"(offK) : CLOB_SCORE);
    DG_OUT
}

DI void out_wave(const Params& p, char* smem, int xg, int csl, int wave, int lane) {
    const int r16 = lane & 15, g = lane >> 4, i4 = r16 >> 2, ig = xg >> 1, cs = 14 * (xg & 1) + csl;
    const unsigned aoff = g * 256 + ((r16 >> 2) & 1) * 128 + (r16 >> 3) * 64 + (r16 & 3) * 16;
    const int k0 = (2 * wave) % 7, k1 = (2 * wave + 1) % 7;
    const unsigned sb = (unsigned)(size_t)smem;
    const unsigned MEML = sb, UL = sb + 8192 + wave * 1024, RED = sb + 13312;
    const bool master = xg == 3 && csl == 13 && wave == 1;
    unsigned* cst = (unsigned*)(smem + 16384 + wave * 9216) + lane;
    cst[0 * 64] = xg * CPY + k0 * 1024 + aoff;
    cst[1 * 64] = xg * CPY + (wave < 3 ? k1 * 1024 + aoff : 7168);
    cst[2 * 64] = MEML + k0 * 1024 + lane * 16;
    cst[3 * 64] = MEML + k1 * 1024 + lane * 16;
#pragma unroll
    for (int sl = 2; sl < 7; ++sl) cst[(2 + sl) * 64] = MEML + ((2 * wave + sl) % 7) * 1024 + lane * 16;
    const unsigned mo = cs * 256 + ig * 128 + (lane & 1) * 64 + ((lane >> 3) & 3) * 16 + ((lane & 7) >> 1) * 4;
    cst[9 * 64] = xg * CPY + (3 + (csl >> 1)) * 16384 + (csl & 1) * 4096 + (4 * wave + g) * 256 + r16 * 8;
    cst[10 * 64] = xg * CPY + 16384 + (4 * wave + g) * 512 + r16 * 8;
    cst[11 * 64] = (lane >> 5) * CPY + mo;
    cst[12 * 64] = UL + ((g >> 1) * 16 + r16) * 16 + (g & 1) * 8;
    cst[13 * 64] = UL + lane * 16;
    cst[14 * 64] = RED + (wave * 64 + lane) * 4;
    cst[15 * 64] = RED + (((lane >> 3) & 3) * 16 + 2 * (lane & 7)) * 4;
    cst[16 * 64] = xg * CPY + 32768 + ((4 * wave + g) * 2 + (r16 & 1)) * 128 + ((r16 & 2) ? 64 + i4 * 4 : ((i4 & 1) * 2 + (i4 >> 1)) * 4);
    cst[17 * 64] = (r16 & 2) ? 0xff800000u : 0x7f800000u;
    cst[18 * 64] = i4 == 0 ? 0xffffu : (i4 == 1 ? 0xffff0000u : 0u);
    cst[19 * 64] = i4 == 2 ? 0xffffu : (i4 == 3 ? 0xffff0000u : 0u);
    cst[20 * 64] = (r16 & 8) ? 0xEEEE : 0x4444;
    cst[22 * 64] = lane * 16;
    cst[23 * 64] = ig == 0 ? lane * 16 : OOR;
    cst[24 * 64] = MEML;
    cst[25 * 64] = (master && lane < 4) ? lane * CPY + 7168 : OOR;
    cst[26 * 64] = lane < 32 ? mo : OOR;
    cst[27 * 64] = 1024 + lane * 4;
    cst[28 * 64] = (wave == 0 && lane == 0) ? CTL_RDY * 4 : OOR;
    cst[29 * 64] = (master && lane == 0) ? CTL_TICK * 4 : OOR;
    RSRC_DECL
    const unsigned ca = sb + 16384 + wave * 9216 + lane * 4;
    const unsigned offW = (unsigned)(OFF_WB + (size_t)((2 * NH + 4 * wave) * NT + cs) * TILE_B);
    const unsigned offU = (unsigned)(OFF_U + (size_t)(wave * NT + cs) * 1024);
    const unsigned offM = (unsigned)OFF_MH + 8192u;
    const int isw0 = __builtin_amdgcn_readfirstlane(wave == 0 ? 1 : 0);
    asm volatile("s_waitcnt lgkmcnt(0)" ::: "memory");
    if (wave < 3) asm volatile(ASM_OUT0 ::COMMON_OPS, "s"(offW), "s"(p.d_smm), "s"(offU), "s"(isw0), "s"(0), "s"(offM) : CLOB_OUT);
    else asm volatile(ASM_OUT1 ::COMMON_OPS, "s"(offW), "s"(p.d_smm), "s"(offU), "s"(isw0), "s"(0), "s"(offM) : CLOB_OUT);
    DG_OUT
}

DI u32x4 ld16(const char* p_) { return *(const u32x4*)p_; }
DI void st16_dev(const Params& p, size_t off, u32x4 v) {
    const __amdgpu_buffer_rsrc_t r = __builtin_amdgcn_make_buffer_rsrc(p.ws, 0, (int)OFF_END, 0x00020000);
    __builtin_amdgcn_raw_buffer_store_b128(v, r, (unsigned)off, 0, 16);
}
#define WAIT_CAP (1u << 17)
DI void wait_ge(gu32* w, unsigned want, int wave) {
    if (wave == 0) {
        unsigned spins = 0;
        while ((unsigned)__builtin_amdgcn_readfirstlane(__hip_atomic_load(w, RLX_AGENT)) < want && ++spins < WAIT_CAP) __builtin_amdgcn_s_sleep(8);
    }
}
DI void acquire_wg(int wave) {
    if (wave == 0) { __builtin_amdgcn_fence(__ATOMIC_ACQUIRE, "agent"); asm volatile("s_waitcnt vmcnt(0)" ::: "memory"); }
    __syncthreads();
}
DI void add1(gu32* w) {
    unsigned long long sv; unsigned one = 1u;
    asm volatile("s_mov_b64 %0, exec\n\ts_mov_b64 exec, 1\n\tglobal_atomic_add %1, %2, off\n\ts_mov_b64 exec, %0" : "=&s"(sv) : "v"(w), "v"(one) : "memory");
}
DI unsigned add1_ret(gu32* w) {
    unsigned long long sv; unsigned one = 1u, r;
    asm volatile("s_mov_b64 %0, exec\n\ts_mov_b64 exec, 1\n\tglobal_atomic_add %1, %2, %3, off sc0\n\ts_waitcnt vmcnt(0)\n\ts_mov_b64 exec, %0" : "=&s"(sv), "=&v"(r) : "v"(w), "v"(one) : "memory");
    return (unsigned)__builtin_amdgcn_readfirstlane(r);
}
DI void signal_wg(gu32* w, int wave) {
    asm volatile("s_waitcnt vmcnt(0)" ::: "memory");
    __syncthreads();
    if (wave == 0) add1(w);
}

DI void gemm_role(const Params& p, char* smem, int gb, int seg0, int stride, int wave, int lane) {
    const int sec = gb / 28, type = sec == 0 ? 0 : (sec == 1 ? 2 : 1);
    const int tl0 = 16 * (gb % 28) + 4 * wave;
    const int h = tl0 / NT, t = tl0 % NT;
    RSRC_DECL
    const unsigned offW = (unsigned)(OFF_WB + (size_t)(type * 448 + tl0) * TILE_B);
    const unsigned offX = (unsigned)OFF_XF + wave * 7168 + seg0 * 28672;
    const unsigned offO = (type == 2 ? (unsigned)(OFF_VO + (size_t)(h * NT + t) * 1024) : (unsigned)(OFF_KQ + (size_t)(h * 2) * TILE_B + (t >> 1) * 1024)) +
                          seg0 * 32u * (unsigned)TILE_B;
    const unsigned sb = __builtin_amdgcn_readfirstlane((unsigned)(size_t)smem);
    const unsigned l16 = lane * 16, w7 = wave * 7168, fo = __builtin_amdgcn_readfirstlane((CTL_SEGC + 64 * sec + seg0) * 4);
    const int isw0 = __builtin_amdgcn_readfirstlane(wave == 0 ? 1 : 0), nit = NSEG / stride;
    (void)c0; (void)c1; (void)c2; (void)c3;
#define RFL(x) __builtin_amdgcn_readfirstlane(x)
#define GEMM_OPS "v"(l16), "s"(w0), "s"(w1), "s"(w2), "s"(w3), "s"(RFL(offW)), "s"(RFL(offX)), "s"(RFL(offO)), "s"(sb), "s"(w7), "s"(fo), "s"(isw0), "s"(stride), "s"(nit)
    if (type == 2) asm volatile(ASM_GEMM1 ::GEMM_OPS : CLOB_GEMM);
    else asm volatile(ASM_GEMM0 ::GEMM_OPS : CLOB_GEMM);
}

DI u32x4 ld16_dev(const Params& p, size_t off) {
    const __amdgpu_buffer_rsrc_t r = __builtin_amdgcn_make_buffer_rsrc(p.ws, 0, (int)OFF_END, 0x00020000);
    return __builtin_amdgcn_raw_buffer_load_b128(r, (unsigned)off, 0, 16);
}
DI unsigned ld4_dev(const Params& p, size_t off) {
    const __amdgpu_buffer_rsrc_t r = __builtin_amdgcn_make_buffer_rsrc(p.ws, 0, (int)OFF_END, 0x00020000);
    return __builtin_amdgcn_raw_buffer_load_b32(r, (unsigned)off, 0, 16);
}
DI void wait_done(gu32* ctl, int wave, int lane) {
    if (wave == 0) {
        const unsigned want = (lane & 7) < 6 ? 16u : 12u;
        unsigned spins = 0;
        while (!__all(__hip_atomic_load(ctl + CTL_DN + 32 * (lane & 7), RLX_AGENT) >= want) && ++spins < WAIT_CAP) __builtin_amdgcn_s_sleep(2);
    }
    lds_barrier();
}
DI void wg_wait(gu32* w, unsigned want, int wave) { wait_ge(w, want, wave); lds_barrier(); }

DI void dma7(const Params& p, unsigned goff, unsigned lds, int wave, int lane) {
    const __amdgpu_buffer_rsrc_t r = __builtin_amdgcn_make_buffer_rsrc(p.ws, 0, (int)OFF_END, 0x00020000);
    const unsigned go = __builtin_amdgcn_readfirstlane(goff + wave * 7168), lo = __builtin_amdgcn_readfirstlane(lds + wave * 7168), l16 = lane * 16;
    unsigned keep, t2;
    asm volatile("s_nop 4\n\ts_mov_b32 %0, m0\n\ts_mov_b32 m0, %4\n\ts_add_u32 %1, %3, 4096\n\t"
                 "buffer_load_dwordx4 %2, %5, %3 offen lds\n\tbuffer_load_dwordx4 %2, %5, %3 offen offset:1024 lds\n\t"
                 "buffer_load_dwordx4 %2, %5, %3 offen offset:2048 lds\n\tbuffer_load_dwordx4 %2, %5, %3 offen offset:3072 lds\n\t"
                 "s_add_u32 m0, %4, 4096\n\ts_nop 0\n\t"
                 "buffer_load_dwordx4 %2, %5, %1 offen lds\n\tbuffer_load_dwordx4 %2, %5, %1 offen offset:1024 lds\n\t"
                 "buffer_load_dwordx4 %2, %5, %1 offen offset:2048 lds\n\ts_mov_b32 m0, %0"
                 : "=&s"(keep), "=&s"(t2) : "v"(l16), "s"(go), "s"(lo), "s"(r) : "memory", "scc");
}
DI void vm_barrier() { __builtin_amdgcn_s_waitcnt(0); asm volatile("s_waitcnt vmcnt(0) lgkmcnt(0)\n\ts_barrier" ::: "memory"); }

#define FT(i)
DI void final_unit(const Params& p, char* smem, int seg, int hg, int mt0, int mt1, int tid, int wave, int lane, gu32* ctl) {
    const int h = 4 * hg + wave;
    const int r16 = lane & 15, g = lane >> 4;
    char* XL = smem;
    char* PL = smem + 32768;
    wait_ge(ctl + CTL_SEGC + seg, 28, wave);
    wg_wait(ctl + CTL_VOC + seg, 28, wave);
    dma7(p, (unsigned)OFF_XF + seg * 28672, (unsigned)(size_t)smem, wave, lane);
    f32x4 sx[2][2];
    u32x4 vf[NT];
    {
        u32x4 kq[2][KS];
#pragma unroll
        for (int kt = 0; kt < 2; ++kt)
#pragma unroll
            for (int ks = 0; ks < KS; ++ks) kq[kt][ks] = ld16_dev(p, OFF_KQ + (size_t)((seg * NH + h) * 2 + kt) * TILE_B + ks * 1024 + lane * 16);
        vm_barrier();
#pragma unroll
        for (int c = 0; c < NT; ++c) vf[c] = ld16_dev(p, OFF_VO + (size_t)((seg * NH + 4 * hg + c / 7) * NT + 7 * wave + c % 7) * 1024 + lane * 16);
#pragma unroll
        for (int m = 0; m < 2; ++m)
#pragma unroll
            for (int kt = 0; kt < 2; ++kt) sx[m][kt] = (f32x4){0.f, 0.f, 0.f, 0.f};
#pragma unroll
        for (int ks = 0; ks < KS; ++ks) {
            const bf16x8 x0 = as8(ld16(XL + ks * 1024 + lane * 16)), x1 = as8(ld16(XL + (KS + ks) * 1024 + lane * 16));
            sx[0][0] = mfma32(as8(kq[0][ks]), x0, sx[0][0]);
            sx[1][0] = mfma32(as8(kq[0][ks]), x1, sx[1][0]);
            sx[1][1] = mfma32(as8(kq[1][ks]), x1, sx[1][1]);
        }
    }
    if (seg <= 60) wg_wait(ctl + CTL_PROG, seg + 3, wave); else wait_done(ctl, wave, lane);
    FT(0)
    f32x4 sm[2];
    u32x4 us[7];
    {
        u32x4 km[16];
#pragma unroll
        for (int i = 0; i < 16; ++i) km[i] = ld16_dev(p, OFF_KM + (size_t)(seg * NH + h) * 16384 + i * 1024 + lane * 16);
#pragma unroll
        for (int i = 0; i < 7; ++i) us[i] = ld16_dev(p, OFF_U + (size_t)((seg * 4 + hg) * NT + 7 * wave + i) * 1024 + lane * 16);
        sm[0] = sm[1] = (f32x4){0.f, 0.f, 0.f, 0.f};
#pragma unroll
        for (int i = 0; i < 16; ++i) {
            const int ks = (((7 * (i >> 2)) & ~1) >> 1) + (i & 3);
            sm[0] = mfma32(as8(km[i]), as8(ld16(XL + ks * 1024 + lane * 16)), sm[0]);
            sm[1] = mfma32(as8(km[i]), as8(ld16(XL + (KS + ks) * 1024 + lane * 16)), sm[1]);
        }
#pragma unroll
        for (int r = 0; r < 4; ++r) { sm[0][r] = swap_add(sm[0][r], sm[0][r]); sm[1][r] = swap_add(sm[1][r], sm[1][r]); }
    }
    FT(1)
#pragma unroll 1
    for (int mt = mt0; mt <= mt1; ++mt) {
        const int qrow = 16 * mt + r16, u = (seg * 2 + mt) * 4 + hg;
        f32x4 sa[3];
        sa[0] = mt ? sm[1] : sm[0]; sa[1] = mt ? sx[1][0] : sx[0][0]; sa[2] = sx[1][1];
        float mx = -3.0e38f;
#pragma unroll
        for (int r = 0; r < 4; ++r) {
            if (g >= 2) sa[0][r] = -3.0e38f;
            if (4 * g + r > qrow) sa[1][r] = -3.0e38f;
            if (16 + 4 * g + r > qrow) sa[2][r] = -3.0e38f;
            mx = fmaxf(mx, fmaxf(sa[0][r], fmaxf(sa[1][r], sa[2][r])));
        }
        mx = xg_max(mx);
        float sum = 0.f;
#pragma unroll
        for (int kt = 0; kt < 3; ++kt)
#pragma unroll
            for (int r = 0; r < 4; ++r) { sa[kt][r] = __builtin_amdgcn_exp2f(sa[kt][r] - mx); sum += sa[kt][r]; }
        sum = xg_sum(sum);
        const float rs = __builtin_amdgcn_rcpf(sum);
        const u32x4 px = {pk2(sa[1][0] * rs, sa[1][1] * rs), pk2(sa[1][2] * rs, sa[1][3] * rs), pk2(sa[2][0] * rs, sa[2][1] * rs), pk2(sa[2][2] * rs, sa[2][3] * rs)};
        *(u32x4*)(PL + (mt * 4 + wave) * 1536 + lane * 16) = px;
        *(u32x2*)(PL + (mt * 4 + wave) * 1536 + 1024 + lane * 8) = (u32x2){pk2(sa[0][0] * rs, sa[0][1] * rs), pk2(sa[0][2] * rs, sa[0][3] * rs)};
        lds_barrier();
        FT(2)
        u32x4 pq[4];
#pragma unroll
        for (int k = 0; k < 4; ++k) pq[k] = ld16(PL + (mt * 4 + k) * 1536 + lane * 16);
        const u32x2 m01 = *(const u32x2*)(PL + (mt * 4 + (g >> 1)) * 1536 + 1024 + (lane & 31) * 8);
        const u32x2 m23 = *(const u32x2*)(PL + (mt * 4 + 2 + (g >> 1)) * 1536 + 1024 + (lane & 31) * 8);
        const u32x4 pf = {m01[0], m01[1], m23[0], m23[1]};
#pragma unroll
        for (int i = 0; i < 7; ++i) {
            f32x4 o = mfma32(as8(us[i]), as8(pf), (f32x4){0.f, 0.f, 0.f, 0.f});
#pragma unroll
            for (int k = 0; k < 4; ++k) o = mfma32(as8(vf[7 * k + i]), as8(pq[k]), o);
            st16_dev(p, OFF_SLAB + (size_t)((u * NT + 7 * wave + i) * 64 + lane) * 16, __builtin_bit_cast(u32x4, o));
        }
    }
    FT(3)
    asm volatile("s_waitcnt vmcnt(0)" ::: "memory");
    int* tk = (int*)(smem + 114688 + 256);
    lds_barrier();
    FT(4)
    if (wave == 0) tk[lane] = (int)add1_ret(ctl + CTL_FT + seg * 2 + mt0);
    lds_barrier();
    FT(5)
    if (mt0 == mt1) {
        if (wave == 0) {
            unsigned spins = 0;
            while ((unsigned)__builtin_amdgcn_readfirstlane(__hip_atomic_load(ctl + CTL_FT + seg * 2 + mt0, RLX_AGENT)) < 4u && ++spins < WAIT_CAP) __builtin_amdgcn_s_sleep(1);
        }
        lds_barrier();
        FT(6)
        u32x4 s[4][2];
#pragma unroll
        for (int k = 0; k < 4; ++k)
#pragma unroll
            for (int i = 0; i < 2; ++i) s[k][i] = ld16_dev(p, OFF_SLAB + (size_t)((((seg * 2 + mt0) * 4 + k) * NT + 7 * hg + min(2 * wave + i, 6)) * 64 + lane) * 16);
#pragma unroll
        for (int i = 0; i < 2; ++i) {
            const f32x4 o = (__builtin_bit_cast(f32x4, s[0][i]) + __builtin_bit_cast(f32x4, s[1][i])) + (__builtin_bit_cast(f32x4, s[2][i]) + __builtin_bit_cast(f32x4, s[3][i]));
            *(u32x2*)(p.out + (size_t)(seg * SEG + 16 * mt0 + r16) * OUTW + 16 * (7 * hg + min(2 * wave + i, 6)) + 4 * g) = (u32x2){pk2(o[0], o[1]), pk2(o[2], o[3])};
        }
    } else if (__builtin_amdgcn_readfirstlane(tk[lane]) == 3) {
#pragma unroll 1
        for (int mt = mt0; mt <= mt1; ++mt) {
            u32x4 s[4][7];
#pragma unroll
            for (int k = 0; k < 4; ++k)
#pragma unroll
                for (int i = 0; i < 7; ++i) s[k][i] = ld16_dev(p, OFF_SLAB + (size_t)((((seg * 2 + mt) * 4 + k) * NT + 7 * wave + i) * 64 + lane) * 16);
#pragma unroll
            for (int i = 0; i < 7; ++i) {
                const f32x4 o = (__builtin_bit_cast(f32x4, s[0][i]) + __builtin_bit_cast(f32x4, s[1][i])) + (__builtin_bit_cast(f32x4, s[2][i]) + __builtin_bit_cast(f32x4, s[3][i]));
                *(u32x2*)(p.out + (size_t)(seg * SEG + 16 * mt + r16) * OUTW + 16 * (7 * wave + i) + 4 * g) = (u32x2){pk2(o[0], o[1]), pk2(o[2], o[3])};
            }
        }
    }
}

constexpr int NB0 = 16;
DI void b0_unit(const Params& p, int h, int wave, int lane) {
    const int r16 = lane & 15, g = lane >> 4;
    u32x4 bw[2], bi[2];
#pragma unroll
    for (int tl = 0; tl < 2; ++tl) {
        unsigned w[8], id[8];
#pragma unroll
        for (int j = 0; j < 8; ++j) {
            const int e = 8 * g + j, f = 16 * tl + r16;
            { const unsigned v = p.bwphi[(h * 28 + min(e, 27)) * 28 + min(f, 27)]; w[j] = (e < 28 && f < 28) ? v : 0u; }
            id[j] = (e == f && e < 28) ? 0x3f80u : 0u;
        }
        bw[tl] = (u32x4){w[0] | (w[1] << 16), w[2] | (w[3] << 16), w[4] | (w[5] << 16), w[6] | (w[7] << 16)};
        bi[tl] = (u32x4){id[0] | (id[1] << 16), id[2] | (id[3] << 16), id[4] | (id[5] << 16), id[6] | (id[7] << 16)};
    }
    u32x4 af[11][2];
    unsigned o[7][8];
#pragma unroll
    for (int k = 0; k < 11; ++k) {
        const int it = min(wave + 4 * k, 3 * KS - 1), type = it / KS, ks = it % KS;
#pragma unroll
        for (int q = 0; q < 2; ++q) {
            const int d = 32 * ks + 8 * (r16 >> 2) + 4 * q + (r16 & 3);
            const u32x2* src = (const u32x2*)(p.bwqkv + (size_t)d * 1344 + 448 * type + 28 * h + 8 * g);
            const u32x2 lo = src[0], hi = src[g < 3 ? 1 : 0];
            af[k][q] = (u32x4){lo[0], lo[1], g < 3 ? hi[0] : 0u, g < 3 ? hi[1] : 0u};
        }
    }
#pragma unroll
    for (int k = 0; k < 7; ++k)
#pragma unroll
        for (int j = 0; j < 8; ++j) {
            const int e = 16 * (j >> 2) + 4 * g + (j & 3);
            const unsigned v = p.bwo[(size_t)(28 * h + min(e, 27)) * DM + 16 * (wave + 4 * k) + r16];
            o[k][j] = e < 28 ? v : 0u;
        }
#pragma unroll
    for (int k = 0; k < 11; ++k) {
        const int it = min(wave + 4 * k, 3 * KS - 1), type = it / KS, ks = it % KS;
        const float sc = type < 2 ? LOG2E : 1.0f;
#pragma unroll
        for (int tl = 0; tl < 2; ++tl) {
            const bf16x8 b = as8(type < 2 ? bw[tl] : bi[tl]);
            const f32x4 lo = mfma32(as8(af[k][0]), b, (f32x4){0.f, 0.f, 0.f, 0.f}), hi = mfma32(as8(af[k][1]), b, (f32x4){0.f, 0.f, 0.f, 0.f});
            st16_dev(p, OFF_BW + (size_t)((type * NH + h) * 2 + tl) * TILE_B + ks * 1024 + lane * 16,
                     (u32x4){pk2(lo[0] * sc, lo[1] * sc), pk2(lo[2] * sc, lo[3] * sc), pk2(hi[0] * sc, hi[1] * sc), pk2(hi[2] * sc, hi[3] * sc)});
        }
    }
#pragma unroll
    for (int k = 0; k < 7; ++k)
        st16_dev(p, OFF_BWO + (size_t)(((wave + 4 * k) * NH + h) * 64 + lane) * 16,
                 (u32x4){o[k][0] | (o[k][1] << 16), o[k][2] | (o[k][3] << 16), o[k][4] | (o[k][5] << 16), o[k][6] | (o[k][7] << 16)});
}

constexpr int NB1 = 16, B1R = 4;
DI void b1_unit(const Params& p, char* smem, int u, int tid, int wave, int lane, gu32* ctl) {
    const int hg = u & 3, rr = u >> 2;
    const int h = 4 * hg + wave, r16 = lane & 15;
    const unsigned xo = (unsigned)OFF_BXF + rr * 16 * 28672, sb = (unsigned)(size_t)smem;
    dma7(p, xo, sb, wave, lane);
    bf16x8 W[4][KS];
#pragma unroll
    for (int a = 0; a < 4; ++a)
#pragma unroll
        for (int ks = 0; ks < KS; ++ks)
            W[a][ks] = as8(ld16_dev(p, OFF_BW + (size_t)(((1 + (a >> 1)) * NH + h) * 2 + (a & 1)) * TILE_B + ks * 1024 + lane * 16));
    vm_barrier();
    f32x4 kv[4][2];
#pragma unroll
    for (int a = 0; a < 4; ++a)
#pragma unroll
        for (int e = 0; e < 2; ++e) kv[a][e] = (f32x4){0.f, 0.f, 0.f, 0.f};
    const bool padl = (lane >> 4) == 3;
    const unsigned sl = (r16 >> 2) == (lane >> 4) ? ((r16 & 1) ? 0x3f800000u : 0x00003f80u) : 0u;
    const unsigned sA = (r16 & 2) ? 0u : sl, sB = (r16 & 2) ? sl : 0u;
    const u32x4 sel0 = {sA, sB, 0u, 0u}, sel1 = {0u, 0u, sA, sB};
#pragma unroll 1
    for (int it = 0; it < 16; ++it) {
        dma7(p, xo + min(it + 1, 15) * 28672, sb + ((it + 1) & 1) * 28672, wave, lane);
        f32x4 acc[2][4];
#pragma unroll
        for (int m = 0; m < 2; ++m)
#pragma unroll
            for (int a = 0; a < 4; ++a) acc[m][a] = (f32x4){0.f, 0.f, 0.f, 0.f};
        const char* xp = smem + (it & 1) * 28672 + lane * 16;
#pragma unroll
        for (int ks = 0; ks < KS; ++ks)
#pragma unroll
            for (int m = 0; m < 2; ++m) {
                const bf16x8 xf = as8(ld16(xp + (m * KS + ks) * 1024));
                acc[m][0] = mfma32(W[0][ks], xf, acc[m][0]);
                acc[m][1] = mfma32(W[1][ks], xf, acc[m][1]);
                acc[m][2] = mfma32(xf, W[2][ks], acc[m][2]);
                acc[m][3] = mfma32(xf, W[3][ks], acc[m][3]);
            }
        f32x4 ph[2][4];
#pragma unroll
        for (int m = 0; m < 2; ++m) {
            float mx = -3.0e38f, mn = 3.0e38f;
#pragma unroll
            for (int r = 0; r < 4; ++r) {
                mx = fmaxf(mx, acc[m][0][r]); mn = fminf(mn, acc[m][0][r]);
                if (!padl) { mx = fmaxf(mx, acc[m][1][r]); mn = fminf(mn, acc[m][1][r]); }
            }
            mx = xg_max(mx); mn = -xg_max(-mn);
            float ea[8], eb[8], sa = 0.f, sb = 0.f;
#pragma unroll
            for (int r = 0; r < 4; ++r) {
                ea[r] = __builtin_amdgcn_exp2f(acc[m][0][r] - mx); eb[r] = __builtin_amdgcn_exp2f(mn - acc[m][0][r]);
                ea[4 + r] = padl ? 0.f : __builtin_amdgcn_exp2f(acc[m][1][r] - mx); eb[4 + r] = padl ? 0.f : __builtin_amdgcn_exp2f(mn - acc[m][1][r]);
            }
#pragma unroll
            for (int j = 0; j < 8; ++j) { sa += ea[j]; sb += eb[j]; }
            const float ra = __builtin_amdgcn_rcpf(xg_sum(sa)), rb = __builtin_amdgcn_rcpf(xg_sum(sb));
            const u32x4 pa = {pk2(ea[0] * ra, ea[1] * ra), pk2(ea[2] * ra, ea[3] * ra), pk2(ea[4] * ra, ea[5] * ra), pk2(ea[6] * ra, ea[7] * ra)};
            const u32x4 pb = {pk2(eb[0] * rb, eb[1] * rb), pk2(eb[2] * rb, eb[3] * rb), pk2(eb[4] * rb, eb[5] * rb), pk2(eb[6] * rb, eb[7] * rb)};
            ph[m][0] = mfma32(as8(pa), as8(sel0), (f32x4){0.f, 0.f, 0.f, 0.f});
            ph[m][1] = mfma32(as8(pa), as8(sel1), (f32x4){0.f, 0.f, 0.f, 0.f});
            ph[m][2] = mfma32(as8(pb), as8(sel0), (f32x4){0.f, 0.f, 0.f, 0.f});
            ph[m][3] = mfma32(as8(pb), as8(sel1), (f32x4){0.f, 0.f, 0.f, 0.f});
            if (r16 == 12) acc[m][3] = (f32x4){1.f, 1.f, 1.f, 1.f};
        }
        u32x4 vb[2];
#pragma unroll
        for (int e = 0; e < 2; ++e)
            vb[e] = (u32x4){pk2(acc[0][2 + e][0], acc[0][2 + e][1]), pk2(acc[0][2 + e][2], acc[0][2 + e][3]), pk2(acc[1][2 + e][0], acc[1][2 + e][1]), pk2(acc[1][2 + e][2], acc[1][2 + e][3])};
#pragma unroll
        for (int a = 0; a < 4; ++a) {
            const u32x4 pa = {pk2(ph[0][a][0], ph[0][a][1]), pk2(ph[0][a][2], ph[0][a][3]), pk2(ph[1][a][0], ph[1][a][1]), pk2(ph[1][a][2], ph[1][a][3])};
#pragma unroll
            for (int e = 0; e < 2; ++e) kv[a][e] = mfma32(as8(pa), as8(vb[e]), kv[a][e]);
        }
        vm_barrier();
    }
#pragma unroll
    for (int a = 0; a < 4; ++a)
#pragma unroll
        for (int e = 0; e < 2; ++e) st16_dev(p, OFF_BKVP + (size_t)(((rr * NH + h) * 8 + a * 2 + e) * 64 + lane) * 16, __builtin_bit_cast(u32x4, kv[a][e]));
    asm volatile("s_waitcnt vmcnt(0)" ::: "memory");
    int* tk = (int*)(smem + 114688 + 256);
    lds_barrier();
    if (wave == 0) tk[lane] = (int)add1_ret(ctl + CTL_B1 + hg);
    lds_barrier();
    if (__builtin_amdgcn_readfirstlane(tk[lane]) == B1R - 1) {
        u32x4 kp[B1R][8];
#pragma unroll
        for (int r = 0; r < B1R; ++r)
#pragma unroll
            for (int k = 0; k < 8; ++k) kp[r][k] = ld16_dev(p, OFF_BKVP + (size_t)(((r * NH + h) * 8 + k) * 64 + lane) * 16);
#pragma unroll
        for (int et = 0; et < 2; ++et)
#pragma unroll
            for (int sg = 0; sg < 2; ++sg) {
                f32x4 lo = {0.f, 0.f, 0.f, 0.f}, hi = {0.f, 0.f, 0.f, 0.f};
#pragma unroll
                for (int r = 0; r < B1R; ++r) { lo += __builtin_bit_cast(f32x4, kp[r][(2 * sg) * 2 + et]); hi += __builtin_bit_cast(f32x4, kp[r][(2 * sg + 1) * 2 + et]); }
                st16_dev(p, OFF_BKV + (size_t)((h * 4 + et * 2 + sg) * 64 + lane) * 16, (u32x4){pk2(lo[0], lo[1]), pk2(lo[2], lo[3]), pk2(hi[0], hi[1]), pk2(hi[2], hi[3])});
            }
        signal_wg(ctl + CTL_B15, wave);
    }
}
constexpr int NB2 = 32;
DI void b2_unit(const Params& p, char* smem, int ub, int tid, int wave, int lane) {
    const int r16 = lane & 15, g = lane >> 4;
    char* XL = smem;
    dma7(p, (unsigned)OFF_BXF + ub * 2 * 28672, (unsigned)(size_t)smem, wave, lane);
    dma7(p, (unsigned)OFF_BXF + (ub * 2 + 1) * 28672, (unsigned)(size_t)smem + 28672, wave, lane);
    u32x4 wq[2 * KS];
#pragma unroll
    for (int k = 0; k < 2 * KS; ++k) wq[k] = ld16_dev(p, OFF_BW + (size_t)(4 * wave * 2) * TILE_B + k * 1024 + lane * 16);
    vm_barrier();
    const bool padl = g == 3;
    u32x4 of[4][4];
#pragma unroll
    for (int hl = 0; hl < 4; ++hl) {
        const int h = 4 * wave + hl;
        bf16x8 kvf[2][2];
#pragma unroll
        for (int et = 0; et < 2; ++et)
#pragma unroll
            for (int sg = 0; sg < 2; ++sg) kvf[et][sg] = as8(ld16_dev(p, OFF_BKV + (size_t)((h * 4 + et * 2 + sg) * 64 + lane) * 16));
        f32x4 acc[4][2];
#pragma unroll
        for (int m = 0; m < 4; ++m)
#pragma unroll
            for (int a = 0; a < 2; ++a) acc[m][a] = (f32x4){0.f, 0.f, 0.f, 0.f};
#pragma unroll
        for (int ks = 0; ks < KS; ++ks)
#pragma unroll
            for (int m = 0; m < 4; ++m) {
                const bf16x8 xf = as8(ld16(XL + (m * KS + ks) * 1024 + lane * 16));
                acc[m][0] = mfma32(as8(wq[ks]), xf, acc[m][0]);
                acc[m][1] = mfma32(as8(wq[KS + ks]), xf, acc[m][1]);
            }
        __builtin_amdgcn_sched_barrier(0);
        if (hl < 3) {
#pragma unroll
            for (int k = 0; k < 2 * KS; ++k) wq[k] = ld16_dev(p, OFF_BW + (size_t)((h + 1) * 2) * TILE_B + k * 1024 + lane * 16);
        }
#pragma unroll
        for (int m = 0; m < 4; ++m) {
            float mx = -3.0e38f, mn = 3.0e38f;
#pragma unroll
            for (int r = 0; r < 4; ++r) {
                mx = fmaxf(mx, acc[m][0][r]); mn = fminf(mn, acc[m][0][r]);
                if (!padl) { mx = fmaxf(mx, acc[m][1][r]); mn = fminf(mn, acc[m][1][r]); }
            }
            mx = xg_max(mx); mn = -xg_max(-mn);
            float ea[8], eb[8], sa = 0.f, sb = 0.f;
#pragma unroll
            for (int r = 0; r < 4; ++r) {
                ea[r] = __builtin_amdgcn_exp2f(acc[m][0][r] - mx); eb[r] = __builtin_amdgcn_exp2f(mn - acc[m][0][r]);
                ea[4 + r] = padl ? 0.f : __builtin_amdgcn_exp2f(acc[m][1][r] - mx); eb[4 + r] = padl ? 0.f : __builtin_amdgcn_exp2f(mn - acc[m][1][r]);
            }
#pragma unroll
            for (int j = 0; j < 8; ++j) { sa += ea[j]; sb += eb[j]; }
            const float ra = __builtin_amdgcn_rcpf(xg_sum(sa)), rb = __builtin_amdgcn_rcpf(xg_sum(sb));
            const u32x4 pa = {pk2(ea[0] * ra, ea[1] * ra), pk2(ea[2] * ra, ea[3] * ra), pk2(ea[4] * ra, ea[5] * ra), pk2(ea[6] * ra, ea[7] * ra)};
            const u32x4 pb = {pk2(eb[0] * rb, eb[1] * rb), pk2(eb[2] * rb, eb[3] * rb), pk2(eb[4] * rb, eb[5] * rb), pk2(eb[6] * rb, eb[7] * rb)};
            f32x4 nm[2];
#pragma unroll
            for (int et = 0; et < 2; ++et) {
                nm[et] = mfma32(kvf[et][0], as8(pa), (f32x4){0.f, 0.f, 0.f, 0.f});
                nm[et] = mfma32(kvf[et][1], as8(pb), nm[et]);
            }
            const float den = xg_sum(padl ? nm[1][0] : 0.f) + 1e-6f;
            const float rd = 1.0f / den;
            of[hl][m] = (u32x4){pk2(nm[0][0] * rd, nm[0][1] * rd), pk2(nm[0][2] * rd, nm[0][3] * rd),
                                padl ? 0u : pk2(nm[1][0] * rd, nm[1][1] * rd), padl ? 0u : pk2(nm[1][2] * rd, nm[1][3] * rd)};
        }
        __builtin_amdgcn_sched_barrier(0);
    }
    u32x4 wf[4][7];
#pragma unroll
    for (int hh = 0; hh < 4; ++hh)
#pragma unroll
        for (int i = 0; i < 7; ++i) wf[hh][i] = ld16_dev(p, OFF_BWO + (size_t)(((7 * wave + i) * NH + hh) * 64 + lane) * 16);
    lds_barrier();
#pragma unroll
    for (int hl = 0; hl < 4; ++hl)
#pragma unroll
        for (int m = 0; m < 4; ++m) *(u32x4*)(smem + ((m * NH + 4 * wave + hl) * 64 + lane) * 16) = of[hl][m];
    lds_barrier();
    f32x4 oa[4][7];
#pragma unroll
    for (int m = 0; m < 4; ++m)
#pragma unroll
        for (int i = 0; i < 7; ++i) oa[m][i] = (f32x4){0.f, 0.f, 0.f, 0.f};
#pragma unroll 1
    for (int hb = 0; hb < 4; ++hb) {
        u32x4 wn[4][7];
        const int hbn = min(hb + 1, 3);
#pragma unroll
        for (int hh = 0; hh < 4; ++hh)
#pragma unroll
            for (int i = 0; i < 7; ++i) wn[hh][i] = ld16_dev(p, OFF_BWO + (size_t)(((7 * wave + i) * NH + 4 * hbn + hh) * 64 + lane) * 16);
#pragma unroll
        for (int hh = 0; hh < 4; ++hh)
#pragma unroll
            for (int m = 0; m < 4; ++m) {
                const bf16x8 o = as8(ld16(smem + ((m * NH + 4 * hb + hh) * 64 + lane) * 16));
#pragma unroll
                for (int i = 0; i < 7; ++i) oa[m][i] = mfma32(as8(wf[hh][i]), o, oa[m][i]);
            }
#pragma unroll
        for (int hh = 0; hh < 4; ++hh)
#pragma unroll
            for (int i = 0; i < 7; ++i) wf[hh][i] = wn[hh][i];
    }
#pragma unroll
    for (int m = 0; m < 4; ++m)
#pragma unroll
        for (int i = 0; i < 7; ++i)
            *(u32x2*)(p.out + (size_t)(ub * 64 + 16 * m + r16) * OUTW + DM + 16 * (7 * wave + i) + 4 * g) = (u32x2){pk2(oa[m][i][0], oa[m][i][1]), pk2(oa[m][i][2], oa[m][i][3])};
}

#define TLOG(id, a, b, c)
#define TNOW() 0u
constexpr int Q_B1 = NB0, Q_B15 = Q_B1 + NB1, Q_B2 = Q_B15, Q_F = Q_B2 + NB2, FSPLIT = 58, Q_F2 = Q_F + 4 * FSPLIT, Q_END = Q_F2 + 8 * (NSEG - FSPLIT);
constexpr int LDS_BYTES = 116736;
DI void task_loop(const Params& p, char* smem, int tid_, int wave, gu32* ctl) {
    int* tk = (int*)(smem + 114688);
#pragma unroll 1
    for (;;) {
        int tid = tid_;
        asm volatile("" : "+v"(tid));
        const int lane = tid & 63;
        if (wave == 0) tk[lane] = (int)add1_ret(ctl + CTL_Q);
        lds_barrier();
        const int t = __builtin_amdgcn_readfirstlane(tk[lane]);
        lds_barrier();
        if (t >= Q_END) break;
        const unsigned tl0 = TNOW(); unsigned tl1 = tl0;
        if (t < Q_B1) {
            b0_unit(p, t, wave, lane);
            signal_wg(ctl + CTL_B0, wave);
        } else if (t < Q_B2) {
            wg_wait(ctl + CTL_B0, NB0, wave); tl1 = TNOW();
            b1_unit(p, smem, t - Q_B1, tid, wave, lane, ctl);
        } else if (t < Q_F) {
            wg_wait(ctl + CTL_B15, 4, wave); tl1 = TNOW();
            for (int i = 0; i < ((t - Q_B2) & 15) * p.d_outmem; ++i) __builtin_amdgcn_s_sleep(8);
            b2_unit(p, smem, t - Q_B2, tid, wave, lane);
        } else {
            const bool big = t < Q_F2;
            const int v = big ? t - Q_F : t - Q_F2, mt = (v >> 2) & 1;
            final_unit(p, smem, big ? v >> 2 : FSPLIT + (v >> 3), v & 3, big ? 0 : mt, big ? 1 : mt, tid, wave, lane, ctl);
        }
        TLOG(t, tl0, tl1, TNOW())
        __builtin_amdgcn_wave_barrier();
    }
}

__global__ void __launch_bounds__(256, 1) k_main(Params p) {
    extern __shared__ __attribute__((aligned(16))) char smem[];
    const int tid = threadIdx.x, lane = tid & 63, wave = __builtin_amdgcn_readfirstlane(tid >> 6);
    const int xcd = blockIdx.x & 7, slot = blockIdx.x >> 3;
    gu32* ctl = (gu32*)(p.ws + OFF_CTL);
    const unsigned tc0 = TNOW();
    if (xcd >= 4) {
        const int bi = slot * 4 + (xcd - 4);
        const unsigned tg0 = TNOW();
#pragma unroll 1
        for (int ph = 0; ph < (bi < 112 ? 1 : 0); ++ph) {
            gemm_role(p, smem, ph == 0 ? bi % 56 : 56 + bi % 28, ph == 0 ? bi / 56 : bi / 28, ph == 0 ? 2 : 4, wave, lane);
            __syncthreads();
        }
        TLOG(640 + bi, tg0, tg0, TNOW())
        __syncthreads();
        if (bi < 112) for (int i = 0; i < (bi & 15) * p.d_smx; ++i) __builtin_amdgcn_s_sleep(16);
        task_loop(p, smem, tid, wave, ctl);
        return;
    }
    if (slot >= 30) {
        if (slot == 30 && wave == 0) {
            __builtin_amdgcn_s_sleep(100);
            unsigned best[2] = {0xffffffffu, 0xffffffffu};
#pragma unroll 1
            for (int rep = 0; rep < 5; ++rep)
#pragma unroll
                for (int par = 0; par < 2; ++par) {
                    const char* q = p.comm + (size_t)xcd * 1048576 + (size_t)(2 + 10 * (rep & 3)) * 16384 + par * 8192 + 4096 + (rep >> 2) * 2048;
                    unsigned v;
                    const unsigned long long t0 = memtime();
                    asm volatile("global_load_dword %0, %1, off sc1\n\ts_waitcnt vmcnt(0)" : "=v"(v) : "v"(q) : "memory");
                    const unsigned long long t1 = memtime();
                    best[par] = min(best[par], (unsigned)(t1 - t0));
                }
            __hip_atomic_store(ctl + CTL_PAR + xcd, best[1] < best[0] ? 2u : 1u, RLX_AGENT);
        }
        if (slot == 31 && xcd == 3 && wave == 0) {
            unsigned last = 0, rdy = 0;
#pragma unroll 1
            for (unsigned spins = 0; spins < (1u << 20) && last < 63u; ++spins) {
                const unsigned tck = (unsigned)__builtin_amdgcn_readfirstlane(__hip_atomic_load(ctl + CTL_TICK, RLX_AGENT));
                if (tck != last) { last = tck; __hip_atomic_store(ctl + CTL_PROG, tck, RLX_AGENT); }
                if (rdy < 64u) {
                    const unsigned a = __hip_atomic_load(ctl + CTL_SEGC + lane, RLX_AGENT), b = __hip_atomic_load(ctl + CTL_VOC + lane, RLX_AGENT);
                    const unsigned long long nr = __ballot(min(a, b) < 28u);
                    const unsigned r = nr ? (unsigned)__builtin_ctzll(nr) : 64u;
                    if (r != rdy) { rdy = r; __hip_atomic_store(ctl + CTL_RDYW, r, RLX_AGENT); }
                }
                __builtin_amdgcn_s_sleep(4);
            }
        }
    } else if (slot < 16) {
        ((unsigned*)(smem + 7168))[tid] = 0u;
        for (int i = tid; i < 1024; i += 256) ((unsigned*)(smem + 8192))[i] = 0u;
        score_wave(p, smem, 4 * xcd + (slot >> 2), slot & 3, xcd, wave, lane);
        signal_wg(ctl + CTL_DN + 32 * (slot & 7), wave);
    } else {
        ((unsigned*)(smem + 7168))[tid] = 0u;
        out_wave(p, smem, xcd, slot - 16, wave, lane);
        signal_wg(ctl + CTL_DN + 32 * (slot & 7), wave);
        TLOG(768 + blockIdx.x, tc0, tc0, TNOW())
    }
}

extern "C" void kernel_launch(void* const* d_in, const int* in_sizes, int n_in, void* d_out, int out_size, void* d_ws, size_t ws_size,
                              hipStream_t stream) {
    Params p;
    p.x = (const unsigned short*)d_in[0]; p.mem0 = (const unsigned short*)d_in[1]; p.Wq = (const unsigned short*)d_in[2];
    p.Wk = (const unsigned short*)d_in[3]; p.Wv = (const unsigned short*)d_in[4]; p.Wo = (const unsigned short*)d_in[5];
    p.bx = (const unsigned short*)d_in[7]; p.bwqkv = (const unsigned short*)d_in[8]; p.bwphi = (const unsigned short*)d_in[9];
    p.bwo = (const unsigned short*)d_in[10];
    p.out = (unsigned short*)d_out; p.ws = (char*)d_ws;
    const size_t a = ((size_t)d_ws + OFF_END + (2u << 20) - 1) & ~(size_t)((2u << 20) - 1);
    p.comm = (char*)a;
    p.d_score = k_dlys[0]; p.d_score_s = k_dlys[1]; p.d_score_k = k_dlys[2]; p.d_smx = k_dlys[3]; p.d_smm = k_dlys[4]; p.d_outmem = k_dlys[5]; p.d_outmem0 = k_dlys[6]; p.pad_ = k_dlys[7];
    (void)hipFuncSetAttribute((const void*)k_main, hipFuncAttributeMaxDynamicSharedMemorySize, LDS_BYTES);
    k0_prep<<<K0_FOLD_WGS + K0_MISC_WGS, 448, 61440, stream>>>(p);
    k_main<<<256, 256, LDS_BYTES, stream>>>(p);
}
```
